```python
import torch

def _softmax(x, axis=-1):
    return torch.softmax(x, dim=axis)
def _gelu(x):
    return torch.nn.functional.gelu(x, approximate="tanh")
def _silu(x):
    return torch.nn.functional.silu(x)
def _sigmoid(x):
    return torch.sigmoid(x)
def _layernorm(x, axis=-1, eps=1e-5):
    m = x.mean(dim=axis, keepdim=True)
    v = x.var(dim=axis, keepdim=True, unbiased=False)
    return (x - m) / torch.sqrt(v + eps)
def _scatter_add(tgt, idx, src, axis=0):
    return tgt.clone().index_add_(axis, idx, src)
def _segment_sum(src, seg, n):
    out = torch.zeros((n,) + src.shape[1:], dtype=src.dtype)
    return out.index_add_(0, seg, src)
def _pad(x, pw):
    flat = tuple(v for pair in reversed(pw) for v in pair)
    return torch.nn.functional.pad(x, flat)

kernel_name = 'standalone__moe_arch_mod__sampling'

def setup_inputs():

    _w = (lambda a: (lambda t: t.to(torch.bfloat16) if t.is_floating_point() else t)(torch.as_tensor(a)))
    B, S, D, F, C = 8, 4, 1024, 3584, 1
    return {
        "x": _w((lambda *s: torch.randn(*s, dtype=torch.bfloat16) * 0.1)(B, S, D)),
        "w_r": _w((lambda *s: torch.randn(*s, dtype=torch.bfloat16) * 0.1)(D)),
        "w_gate": _w((lambda *s: torch.randn(*s, dtype=torch.bfloat16) * 0.1)(D, F)),
        "w_up": _w((lambda *s: torch.randn(*s, dtype=torch.bfloat16) * 0.1)(D, F)),
        "w_down": _w((lambda *s: torch.randn(*s, dtype=torch.bfloat16) * 0.1)(F, D)),
    }

def reference(x, w_r, w_gate, w_up, w_down):
    B, S, D = x.shape
    C = 1
    r = x @ w_r
    idx = torch.argsort(-r, axis=-1)[:, :C]
    idx_e = (lambda x, n, axis: torch.repeat_interleave(x, n, dim=axis))(idx.reshape(B, C, 1), D, axis=2)
    x_sel = (lambda x, i, axis: torch.take_along_dim(x, i, dim=axis))(x, idx_e, axis=1)
    g = _silu(x_sel @ w_gate)
    u = x_sel @ w_up
    y = (g * u) @ w_down
    r_sel = _sigmoid((lambda x, i, axis: torch.take_along_dim(x, i, dim=axis))(r, idx, axis=1))
    y = y * r_sel.reshape(B, C, 1)
    b_off = (torch.arange(B) * S).reshape(B, 1)
    flat_idx = (idx + b_off).reshape(B * C)
    zero = (x * 0.0).reshape(B * S, D)
    upd = _scatter_add(zero, flat_idx, y.reshape(B * C, D), axis=0)
    return x + upd.reshape(B, S, D)
```

```cpp
#include <hip/hip_runtime.h>
#include <hip/hip_bf16.h>
#include <stdint.h>

constexpr int B = 8, S = 4, D = 1024, F = 3584;
typedef unsigned short u16;

__device__ __forceinline__ float bf2f(u16 v) { return __uint_as_float(((unsigned)v) << 16); }
__device__ __forceinline__ u16 f2bf(float f) { __hip_bfloat16 h = __float2bfloat16(f); return *(u16*)&h; }
__device__ __forceinline__ float rbf(float f) { return bf2f(f2bf(f)); }

__device__ void router(const u16* x, const u16* wr, float* r_s, int* sel_s) {
  int tid = threadIdx.x, lane = tid & 63, w = tid >> 6, nw = blockDim.x >> 6;
  for (int t = w; t < B * S; t += nw) {
    float a = 0.f;
    for (int k = lane; k < D; k += 64) a += bf2f(x[t * D + k]) * bf2f(wr[k]);
    for (int o = 32; o; o >>= 1) a += __shfl_xor(a, o);
    if (lane == 0) r_s[t] = rbf(a);
  }
  __syncthreads();
  if (tid < B) {
    int best = 0; float bv = r_s[tid * S];
    for (int s = 1; s < S; ++s) { float v = r_s[tid * S + s]; if (v > bv) { bv = v; best = s; } }
    sel_s[tid] = best;
  }
  __syncthreads();
}

__global__ void __launch_bounds__(256) k1(const u16* x, const u16* wr, const u16* wg, const u16* wu, u16* h) {
  __shared__ float r_s[32]; __shared__ int sel_s[8];
  __shared__ float red[2][16][8][16];
  router(x, wr, r_s, sel_s);
  int tid = threadIdx.x, c = tid & 15, ks = tid >> 4;
  int col = blockIdx.x * 16 + c;
  float g[8], u[8];
  for (int m = 0; m < 8; ++m) { g[m] = 0; u[m] = 0; }
  for (int k = ks; k < D; k += 16) {
    float a = bf2f(wg[(size_t)k * F + col]), b = bf2f(wu[(size_t)k * F + col]);
#pragma unroll
    for (int m = 0; m < 8; ++m) { float xv = bf2f(x[(m * S + sel_s[m]) * D + k]); g[m] += xv * a; u[m] += xv * b; }
  }
  for (int m = 0; m < 8; ++m) { red[0][ks][m][c] = g[m]; red[1][ks][m][c] = u[m]; }
  __syncthreads();
  if (tid < 128) {
    int m = tid >> 4; float gg = 0, uu = 0;
    for (int i = 0; i < 16; ++i) { gg += red[0][i][m][c]; uu += red[1][i][m][c]; }
    gg = rbf(gg); uu = rbf(uu);
    float s = rbf(gg / (1.f + expf(-gg)));
    h[m * F + col] = f2bf(s * uu);
  }
}

__global__ void __launch_bounds__(256) k2(const u16* x, const u16* wr, const u16* wd, const u16* h, u16* out) {
  __shared__ float r_s[32]; __shared__ int sel_s[8];
  __shared__ float red[64][8][4];
  router(x, wr, r_s, sel_s);
  int tid = threadIdx.x, c = tid & 3, ks = tid >> 2;
  int col = blockIdx.x * 4 + c;
  float y[8];
  for (int m = 0; m < 8; ++m) y[m] = 0;
  for (int k = ks; k < F; k += 64) {
    float a = bf2f(wd[(size_t)k * D + col]);
#pragma unroll
    for (int m = 0; m < 8; ++m) y[m] += bf2f(h[m * F + k]) * a;
  }
  for (int m = 0; m < 8; ++m) red[ks][m][c] = y[m];
  __syncthreads();
  if (tid < 128) {
    int t = tid >> 2, m = t >> 2, s = t & 3;
    float xv = bf2f(x[t * D + col]);
    float upd = xv * 0.0f;
    if (s == sel_s[m]) {
      float yy = 0;
      for (int i = 0; i < 64; ++i) yy += red[i][m][c];
      float rs = rbf(1.f / (1.f + expf(-r_s[t])));
      upd = rbf(upd + rbf(rbf(yy) * rs));
    }
    out[t * D + col] = f2bf(xv + upd);
  }
}

extern "C" void kernel_launch(void* const* d_in, const int* in_sizes, int n_in,
                              void* d_out, int out_size, void* d_ws, size_t ws_size,
                              hipStream_t stream) {
  const u16* x = (const u16*)d_in[0]; const u16* wr = (const u16*)d_in[1];
  const u16* wg = (const u16*)d_in[2]; const u16* wu = (const u16*)d_in[3]; const u16* wd = (const u16*)d_in[4];
  u16* h = (u16*)d_ws;
  k1<<<F / 16, 256, 0, stream>>>(x, wr, wg, wu, h);
  k2<<<D / 4, 256, 0, stream>>>(x, wr, wd, h, (u16*)d_out);
}
```

```cpp
#include <hip/hip_runtime.h>
#include <stdint.h>

#define WAUX " nt"
#define THR 12
#define GREM 4

namespace {
typedef unsigned short u16;
typedef unsigned long long u64;
typedef unsigned v4u __attribute__((ext_vector_type(4)));
typedef unsigned v2u __attribute__((ext_vector_type(2)));
typedef float f32x2 __attribute__((ext_vector_type(2)));
typedef float f32x4 __attribute__((ext_vector_type(4)));
typedef float f32x16 __attribute__((ext_vector_type(16)));
typedef short s16x4 __attribute__((ext_vector_type(4)));
typedef __bf16 bf16x2 __attribute__((ext_vector_type(2)));
typedef __bf16 bf16x8 __attribute__((ext_vector_type(8)));
#define GAS __attribute__((address_space(1)))
#define LAS __attribute__((address_space(3)))
#define CAS __attribute__((address_space(4)))
#define RLX_AGENT __ATOMIC_RELAXED, __HIP_MEMORY_SCOPE_AGENT

constexpr int D = 1024, F = 3584;
constexpr int NROUTER = 8, NGROUP = 56, GRID = NROUTER + 4 * NGROUP;
constexpr int W_BYTES = D * F * 2, X_BYTES = 32 * D * 2;
constexpr int WS_SEL = 1024, WS_P1 = 4096, WS_P2 = WS_P1 + 4 * NGROUP * 4096, WS_BYTES = WS_P2 + NGROUP * 32768;
constexpr int L_W1 = 0, L_WD = 65536, L_X = 98304, L_H = 114688, H_ROW = 144, L_Z = L_H + 8 * H_ROW, L_SEL = L_Z + 128,
              L_BYTES = L_SEL + 64;
constexpr int AUX_SC1 = 16;
constexpr unsigned SPIN_CAP = 1u << 16;

#define STAMP(i)
#define STAMP_OUT()

__device__ __forceinline__ v4u srd(const void* p, unsigned bytes) {
  const u64 a = (u64)p;
  return v4u{(unsigned)a, (unsigned)(a >> 32) & 0xffffu, bytes, 0x00020000u};
}
template <int LOFF, int GOFF> __device__ __forceinline__ void dma_w(unsigned lds, unsigned voff, v4u rs) {
  unsigned t;
  asm volatile("s_add_u32 m0, %1, %4\n\ts_mov_b32 %0, %5\n\tbuffer_load_dwordx4 %2, %3, %0 offen" WAUX " lds"
               : "=&s"(t) : "s"(lds), "v"(voff), "s"(rs), "i"(LOFF), "i"(GOFF) : "scc", "memory");
}
template <int LOFF, int GOFF> __device__ __forceinline__ void dma_x(unsigned lds, unsigned voff, v4u rs) {
  unsigned t;
  asm volatile("s_add_u32 m0, %1, %4\n\ts_mov_b32 %0, %5\n\tbuffer_load_dwordx4 %2, %3, %0 offen lds"
               : "=&s"(t) : "s"(lds), "v"(voff), "s"(rs), "i"(LOFF), "i"(GOFF) : "scc", "memory");
}
template <int N> __device__ __forceinline__ void wait_vm() { asm volatile("s_waitcnt vmcnt(%0)" ::"i"(N) : "memory"); }
__device__ __forceinline__ void wg_barrier() { asm volatile("s_waitcnt lgkmcnt(0)\n\ts_barrier" ::: "memory"); }

__device__ __forceinline__ bf16x8 tr_frag(LAS char* p) {
  const s16x4 lo = __builtin_amdgcn_ds_read_tr16_b64_v4i16((LAS s16x4*)p);
  const s16x4 hi = __builtin_amdgcn_ds_read_tr16_b64_v4i16((LAS s16x4*)(p + 512));
  return __builtin_bit_cast(bf16x8, __builtin_shufflevector(lo, hi, 0, 1, 2, 3, 4, 5, 6, 7));
}
__device__ __forceinline__ unsigned pack_bf16(float a, float b) {
  return __builtin_bit_cast(unsigned, __builtin_convertvector(f32x2{a, b}, bf16x2));
}
__device__ __forceinline__ unsigned as_u(float v) { return __builtin_bit_cast(unsigned, v); }
__device__ __forceinline__ float as_f(unsigned v) { return __builtin_bit_cast(float, v); }
__device__ __forceinline__ float bf_lo(unsigned p) { return __builtin_bit_cast(float, p << 16); }
__device__ __forceinline__ float bf_hi(unsigned p) { return __builtin_bit_cast(float, p & 0xffff0000u); }
__device__ __forceinline__ float sigmoidf(float v) { return __builtin_amdgcn_rcpf(1.0f + __expf(-v)); }

__device__ __forceinline__ v4u tag_chunk(float a, float b, float c, float d, v4u tb) {
  return v4u{(__builtin_bit_cast(unsigned, a) & 0xffffff00u) | tb.x, (__builtin_bit_cast(unsigned, b) & 0xffffff00u) | tb.y,
             (__builtin_bit_cast(unsigned, c) & 0xffffff00u) | tb.z, (__builtin_bit_cast(unsigned, d) & 0xffffff00u) | tb.w};
}
template <int N, int STRIDE>
__device__ __forceinline__ f32x4 gather_sum(__amdgpu_buffer_rsrc_t rs, int off, v4u tb, bool& ok) {
  f32x4 sum;
#pragma nounroll
  for (unsigned spins = 0;; ++spins) {
    v4u c[N];
#pragma unroll
    for (int i = 0; i < N; ++i) c[i] = __builtin_amdgcn_raw_buffer_load_b128(rs, off, i * STRIDE, AUX_SC1);
    unsigned bad = 0;
#pragma unroll
    for (int i = 0; i < N; ++i) {
      c[i] ^= tb;
      bad |= c[i].x | c[i].y | c[i].z | c[i].w;
      sum = i ? sum + __builtin_bit_cast(f32x4, c[i]) : __builtin_bit_cast(f32x4, c[i]);
    }
    if (__builtin_amdgcn_ballot_w64((bad & 0xffu) != 0) == 0) break;
    if (spins > SPIN_CAP) { ok = false; break; }
    asm volatile("" ::: "memory");
  }
  return sum;
}

template <int I> __device__ __forceinline__ void stream_item(unsigned l1, unsigned v1, v4u r1, unsigned ld, unsigned vd0, unsigned vd1, v4u rd) {
  if constexpr (I < 32) dma_w<I * 1024, I * 8 * F * 2>(l1, v1, r1);
  else if constexpr (I < 40) dma_w<(I - 32) * 1024, (I - 32) * 8 * D * 2>(ld, (I & 1) ? vd1 : vd0, rd);
}
template <int LO, int HI> __device__ __forceinline__ void stream_range(unsigned l1, unsigned v1, v4u r1, unsigned ld, unsigned vd0, unsigned vd1, v4u rd) {
  if constexpr (LO < HI) { stream_item<LO>(l1, v1, r1, ld, vd0, vd1, rd); stream_range<LO + 1, HI>(l1, v1, r1, ld, vd0, vd1, rd); }
}
template <int SS> __device__ __forceinline__ void phase1_steps(unsigned l1, unsigned v1, v4u r1, unsigned ld, unsigned vd0, unsigned vd1, v4u rd,
                                                               LAS char* a0, LAS char* a1, const bf16x8 (&xf)[16], f32x16& c0, f32x16& c1) {
  if constexpr (SS < 16) {
    constexpr int issued = THR + 2 * SS < 40 ? THR + 2 * SS : 40;
    wait_vm<issued - (2 * SS + 2)>();
    stream_range<THR + 2 * SS, THR + 2 * SS + 2>(l1, v1, r1, ld, vd0, vd1, rd);
    if constexpr (THR + 2 * SS == 30) wg_barrier();
    c0 = __builtin_amdgcn_mfma_f32_32x32x16_bf16(tr_frag(a0 + SS * 2048), xf[SS], c0, 0, 0, 0);
    c1 = __builtin_amdgcn_mfma_f32_32x32x16_bf16(tr_frag(a1 + SS * 2048), xf[SS], c1, 0, 0, 0);
    phase1_steps<SS + 1>(l1, v1, r1, ld, vd0, vd1, rd, a0, a1, xf, c0, c1);
  }
}
template <int I> __device__ __forceinline__ void x_range(unsigned lx, unsigned vx, v4u rx) {
  if constexpr (I < 8) { dma_x<I * 1024, I * 2 * D * 2>(lx, vx ^ (I * 32), rx); x_range<I + 1>(lx, vx, rx); }
}
static_assert(THR % 2 == 0 && THR >= 8 && THR <= 30, "the step that issues the last gate/up piece holds the second barrier");

__device__ __forceinline__ float wave_sum(float v) {
#pragma unroll
  for (int o = 32; o; o >>= 1) v += __shfl_xor(v, o);
  return v;
}

__global__ void __launch_bounds__(256, 1)
mod_ffn_kernel(const u16* __restrict__ x, const u16* __restrict__ wr, const u16* __restrict__ wg, const u16* __restrict__ wu,
               const u16* __restrict__ wd, u16* __restrict__ out, char* ws) {
  __shared__ __attribute__((aligned(1024))) char smem_[L_BYTES];
  LAS char* smem = (LAS char*)smem_;
  const int lane = threadIdx.x & 63, wv = __builtin_amdgcn_readfirstlane(threadIdx.x >> 6);
  STAMP(0);
  unsigned tag = *(const CAS unsigned*)ws + 0x01010101u;

  if (blockIdx.x < NROUTER) {
    if (wv != 0) return;
    const int b = blockIdx.x;
    const GAS v4u* xp = (const GAS v4u*)(x + b * 4 * D) + lane;
    const GAS v4u* wp = (const GAS v4u*)wr + lane;
    v4u xv[4][2], wv2[2];
#pragma unroll
    for (int s = 0; s < 4; ++s) { xv[s][0] = xp[s * 128]; xv[s][1] = xp[s * 128 + 64]; }
    wv2[0] = wp[0]; wv2[1] = wp[64];
    int sel = 0; float best = 0.f;
#pragma unroll
    for (int s = 0; s < 4; ++s) {
      float a = 0.f;
#pragma unroll
      for (int i = 0; i < 2; ++i)
#pragma unroll
        for (int e = 0; e < 4; ++e) { a = fmaf(bf_lo(xv[s][i][e]), bf_lo(wv2[i][e]), a); a = fmaf(bf_hi(xv[s][i][e]), bf_hi(wv2[i][e]), a); }
      const float r = bf_lo(pack_bf16(wave_sum(a), 0.f));
      const bool take = s == 0 || r > best || (best != best && r == r);
      sel = take ? s : sel; best = take ? r : best;
    }
    STAMP(1);
    if (lane == 0)
      __hip_atomic_store((GAS u64*)(ws + WS_SEL) + b, ((u64)tag << 32) | (__builtin_bit_cast(unsigned, best) & 0xffff0000u) | (unsigned)sel, RLX_AGENT);
    GAS v4u* op = (GAS v4u*)(out + b * 4 * D) + lane;
#pragma unroll
    for (int s = 0; s < 4; ++s)
      if (s != sel) {
#pragma unroll
        for (int i = 0; i < 2; ++i) {
          v4u o;
#pragma unroll
          for (int e = 0; e < 4; ++e) { const float lo = bf_lo(xv[s][i][e]), hi = bf_hi(xv[s][i][e]); o[e] = pack_bf16(lo + lo * 0.0f, hi + hi * 0.0f); }
          op[s * 128 + i * 64] = o;
        }
      }
    STAMP_OUT();
    return;
  }

  const int wk = blockIdx.x - NROUTER, j = wk / NGROUP, g = wk - j * NGROUP;
  const unsigned lds0 = (unsigned)(size_t)smem;
  const v4u rd = srd(wd, W_BYTES);
  const unsigned sw = (lane & 7) ^ ((lane >> 2) & 4);
  const unsigned vd0 = (g * 64 + (lane >> 3)) * (D * 2) + j * 512 + wv * 128 + sw * 16, vd1 = vd0 ^ 32;
  const unsigned ld = lds0 + L_WD + wv * 8192;
  const __amdgpu_buffer_rsrc_t rw = __builtin_amdgcn_make_buffer_rsrc((void*)ws, 0, WS_BYTES, 0x00020000);
  const int q = (lane >> 2) & 3, p = lane & 3, G = lane >> 4;
  bool ok = true;
  v4u tb;

  if (wv < 2) {
    const v4u r1 = srd(wv ? wu : wg, W_BYTES);
    const unsigned v1 = (j * 256 + (lane >> 3)) * (F * 2) + g * 128 + sw * 16, l1 = lds0 + L_W1 + wv * 32768;
    stream_range<0, THR>(l1, v1, r1, ld, vd0, vd1, rd);
    STAMP(1);
    asm volatile("" : "+s"(tag));
    tb = v4u{tag & 255u, (tag >> 8) & 255u, (tag >> 16) & 255u, tag >> 24};
    wg_barrier();
    STAMP(2);
    bf16x8 xf[16];
    const unsigned xa = (lane & 31) * 512 + (((lane >> 5) ^ (lane & 15)) * 16);
#pragma unroll
    for (int s = 0; s < 16; ++s) xf[s] = *(LAS bf16x8*)(smem + L_X + (xa ^ (s * 32)));
    const unsigned ta = (8 * (G >> 1) + q) * 128 + ((32 * (G & 1) + 8 * p) ^ (64 * (q >> 1)));
    f32x16 c0 = {}, c1 = {};
    phase1_steps<0>(l1, v1, r1, ld, vd0, vd1, rd, smem + L_W1 + wv * 32768 + ta, smem + L_W1 + wv * 32768 + (ta ^ 64), xf, c0, c1);
    STAMP(3);
    const int b = (lane & 31) >> 2;
    if ((*(LAS unsigned*)(smem + L_SEL + b * 4) & 3u) == (unsigned)(lane & 3)) {
      const int off = WS_P1 + (g * 4 + j) * 4096 + wv * 2048 + (b * 2 + (lane >> 5)) * 16;
#pragma unroll
      for (int k = 0; k < 4; ++k) {
        __builtin_amdgcn_raw_buffer_store_b128(tag_chunk(c0[4 * k], c0[4 * k + 1], c0[4 * k + 2], c0[4 * k + 3], tb), rw, off + k * 256, 0, AUX_SC1);
        __builtin_amdgcn_raw_buffer_store_b128(tag_chunk(c1[4 * k], c1[4 * k + 1], c1[4 * k + 2], c1[4 * k + 3], tb), rw, off + 1024 + k * 256, 0, AUX_SC1);
      }
    }
    STAMP(4);
    wait_vm<8 + GREM>();
  } else {
    const v4u rx = srd(x, X_BYTES);
    const int t0 = (wv - 2) * 16 + (lane >> 5);
    x_range<0>(lds0 + L_X + (wv - 2) * 8192, t0 * (D * 2) + j * 512 + (((lane & 31) ^ (lane >> 5)) * 16), rx);
    if (lane < 32) *(LAS unsigned*)(smem + L_Z + lane * 4) = 0u;
    asm volatile("" : "+s"(tag));
    tb = v4u{tag & 255u, (tag >> 8) & 255u, (tag >> 16) & 255u, tag >> 24};
    wait_vm<0>();
    STAMP(1);
    wg_barrier();
    if (wv == 2) {
#pragma nounroll
      for (unsigned spins = 0;; ++spins) {
        const u64 v = __hip_atomic_load((GAS u64*)(ws + WS_SEL) + (lane & 7), RLX_AGENT);
        if (__builtin_amdgcn_ballot_w64((unsigned)(v >> 32) != tag) == 0) { if (lane < 8) *(LAS unsigned*)(smem + L_SEL + lane * 4) = (unsigned)v; break; }
        if (spins > SPIN_CAP) { ok = false; break; }
      }
    }
    STAMP(2);
    wg_barrier();
    STAMP(3);
    stream_range<32, 40>(0, 0, rd, ld, vd0, vd1, rd);
  }
  wg_barrier();
  STAMP(5);

  const int pos = wv * 32 + (lane & 31);
  const f32x4 s1 = gather_sum<4, 4096>(rw, WS_P1 + g * 16384 + (lane >> 5) * 2048 + pos * 16, tb, ok);
  STAMP(6);
  wait_vm<0>();
  bf16x8 wf[4][2];
  {
    const unsigned da = (8 * G + q) * 128 + ((8 * p) ^ (64 * (q >> 1)) ^ (32 * (G & 1)));
#pragma unroll
    for (int ct = 0; ct < 4; ++ct)
#pragma unroll
      for (int ks = 0; ks < 2; ++ks) wf[ct][ks] = tr_frag(smem + L_WD + wv * 8192 + (da ^ (32 * ct)) + ks * 4096);
  }
  {
    const v2u e = __builtin_amdgcn_permlane32_swap(as_u(s1[0]), as_u(s1[2]), false, false);
    const v2u o = __builtin_amdgcn_permlane32_swap(as_u(s1[1]), as_u(s1[3]), false, false);
    const unsigned gp = pack_bf16(as_f(e.x), as_f(o.x));
    const unsigned up = pack_bf16(as_f(e.y), as_f(o.y));
    const float g0 = bf_lo(gp), g1 = bf_hi(gp);
    const unsigned sp = pack_bf16(g0 * sigmoidf(g0), g1 * sigmoidf(g1));
    const unsigned hp = pack_bf16(bf_lo(sp) * bf_lo(up), bf_hi(sp) * bf_hi(up));
    const int col = (pos >> 4) * 8 + (pos & 1) * 4 + (lane >> 5) * 2;
    *(LAS unsigned*)(smem + L_H + ((pos >> 1) & 7) * H_ROW + col * 2) = hp;
  }
  wg_barrier();
  STAMP(7);

  {
    const unsigned ha = L_H + (lane & 7) * H_ROW + G * 16;
    LAS char* plo = smem + ((lane & 8) ? L_Z : ha);
    LAS char* phi = smem + ((lane & 8) ? ha : L_Z);
    const bf16x8 lo0 = *(LAS bf16x8*)plo, lo1 = *(LAS bf16x8*)(plo + 64), hi0 = *(LAS bf16x8*)phi, hi1 = *(LAS bf16x8*)(phi + 64);
    const int off = WS_P2 + g * 32768 + (j * 8 + wv * 2) * 1024 + (lane & 7) * 128 + (lane & 8) * 8 + G * 16;
#pragma unroll
    for (int a = 0; a < 2; ++a) {
      f32x4 c = {};
      c = __builtin_amdgcn_mfma_f32_16x16x32_bf16(wf[2 * a][0], lo0, c, 0, 0, 0);
      c = __builtin_amdgcn_mfma_f32_16x16x32_bf16(wf[2 * a][1], lo1, c, 0, 0, 0);
      c = __builtin_amdgcn_mfma_f32_16x16x32_bf16(wf[2 * a + 1][0], hi0, c, 0, 0, 0);
      c = __builtin_amdgcn_mfma_f32_16x16x32_bf16(wf[2 * a + 1][1], hi1, c, 0, 0, 0);
      __builtin_amdgcn_raw_buffer_store_b128(tag_chunk(c[0], c[1], c[2], c[3], tb), rw, off + a * 1024, 0, AUX_SC1);
    }
  }
  STAMP(8);

  if (wv == 2 || (wv == 3 && g < 8)) {
    const int t = g + (wv - 2) * NGROUP, m = t & 7, cb = t >> 3;
    const unsigned sv = *(LAS unsigned*)(smem + L_SEL + m * 4);
    const int tok = m * 4 + (sv & 3u);
    const int c8 = lane & 7;
    const v2u xr = *(LAS v2u*)(smem + L_X + tok * 512 + (((cb * 4 + (c8 >> 1)) ^ (tok & 15)) * 16) + (c8 & 1) * 8);
    const float rs = bf_lo(pack_bf16(sigmoidf(bf_hi(sv)), 0.f));
    f32x4 y = gather_sum<7, 8 * 32768>(rw, WS_P2 + (lane >> 3) * 32768 + (j * 8 + cb) * 1024 + m * 128 + c8 * 16, tb, ok);
    STAMP(9);
#pragma unroll
    for (int e = 0; e < 4; ++e) { y[e] += __shfl_xor(y[e], 8); y[e] += __shfl_xor(y[e], 16); y[e] += __shfl_xor(y[e], 32); }
    v2u o;
#pragma unroll
    for (int e = 0; e < 2; ++e) {
      const unsigned yp = pack_bf16(y[2 * e], y[2 * e + 1]);
      const unsigned pp = pack_bf16(bf_lo(yp) * rs, bf_hi(yp) * rs);
      const float x0 = bf_lo(xr[e]), x1 = bf_hi(xr[e]);
      o[e] = pack_bf16(x0 + (x0 * 0.0f + bf_lo(pp)), x1 + (x1 * 0.0f + bf_hi(pp)));
    }
    if (lane < 8) *(GAS v2u*)(out + tok * D + j * 256 + cb * 32 + c8 * 4) = o;
    if (wk == 0 && wv == 2 && lane == 0 && ok) __hip_atomic_store((GAS unsigned*)ws, tag, RLX_AGENT);
    STAMP(10);
  }
  STAMP_OUT();
}
}

extern "C" void kernel_launch(void* const* d_in, const int* in_sizes, int n_in, void* d_out, int out_size, void* d_ws, size_t ws_size,
                              hipStream_t stream) {
  mod_ffn_kernel<<<GRID, 256, 0, stream>>>((const u16*)d_in[0], (const u16*)d_in[1], (const u16*)d_in[2], (const u16*)d_in[3],
                                           (const u16*)d_in[4], (u16*)d_out, (char*)d_ws);
}
```

```cpp
#include <hip/hip_runtime.h>
#include <stdint.h>

#define WAUX " nt"
#define THR 12
#define GREM 4

namespace {
typedef unsigned short u16;
typedef unsigned long long u64;
typedef unsigned v4u __attribute__((ext_vector_type(4)));
typedef unsigned v2u __attribute__((ext_vector_type(2)));
typedef float f32x2 __attribute__((ext_vector_type(2)));
typedef float f32x4 __attribute__((ext_vector_type(4)));
typedef float f32x16 __attribute__((ext_vector_type(16)));
typedef short s16x4 __attribute__((ext_vector_type(4)));
typedef __bf16 bf16x2 __attribute__((ext_vector_type(2)));
typedef __bf16 bf16x8 __attribute__((ext_vector_type(8)));
#define GAS __attribute__((address_space(1)))
#define LAS __attribute__((address_space(3)))
#define CAS __attribute__((address_space(4)))
#define RLX_AGENT __ATOMIC_RELAXED, __HIP_MEMORY_SCOPE_AGENT

constexpr int D = 1024, F = 3584;
constexpr int NROUTER = 8, NGROUP = 56, GRID = NROUTER + 4 * NGROUP;
constexpr int W_BYTES = D * F * 2, X_BYTES = 32 * D * 2;
constexpr int WS_SEL = 1024, WS_P1 = 4096, WS_P2 = WS_P1 + 4 * NGROUP * 4096, WS_BYTES = WS_P2 + NGROUP * 32768;
constexpr int L_W1 = 0, L_WD = 65536, L_X = 98304, L_H = 114688, H_ROW = 144, L_Z = L_H + 8 * H_ROW, L_SEL = L_Z + 128,
              L_BYTES = L_SEL + 64;
constexpr int AUX_SC1 = 16;
constexpr unsigned SPIN_CAP = 1u << 16;

#define STAMP(i)
#define STAMP_OUT()

__device__ __forceinline__ v4u srd(const void* p, unsigned bytes) {
  const u64 a = (u64)p;
  return v4u{(unsigned)a, (unsigned)(a >> 32) & 0xffffu, bytes, 0x00020000u};
}
template <int LOFF, int GOFF> __device__ __forceinline__ void dma_w(unsigned lds, unsigned voff, v4u rs) {
  unsigned t;
  asm volatile("s_add_u32 m0, %1, %4\n\ts_mov_b32 %0, %5\n\tbuffer_load_dwordx4 %2, %3, %0 offen" WAUX " lds"
               : "=&s"(t) : "s"(lds), "v"(voff), "s"(rs), "i"(LOFF), "i"(GOFF) : "scc", "memory");
}
template <int LOFF, int GOFF> __device__ __forceinline__ void dma_x(unsigned lds, unsigned voff, v4u rs) {
  unsigned t;
  asm volatile("s_add_u32 m0, %1, %4\n\ts_mov_b32 %0, %5\n\tbuffer_load_dwordx4 %2, %3, %0 offen lds"
               : "=&s"(t) : "s"(lds), "v"(voff), "s"(rs), "i"(LOFF), "i"(GOFF) : "scc", "memory");
}
template <int N> __device__ __forceinline__ void wait_vm() { asm volatile("s_waitcnt vmcnt(%0)" ::"i"(N) : "memory"); }
__device__ __forceinline__ void wg_barrier() { asm volatile("s_waitcnt lgkmcnt(0)\n\ts_barrier" ::: "memory"); }

__device__ __forceinline__ bf16x8 tr_frag(LAS char* p) {
  const s16x4 lo = __builtin_amdgcn_ds_read_tr16_b64_v4i16((LAS s16x4*)p);
  const s16x4 hi = __builtin_amdgcn_ds_read_tr16_b64_v4i16((LAS s16x4*)(p + 512));
  return __builtin_bit_cast(bf16x8, __builtin_shufflevector(lo, hi, 0, 1, 2, 3, 4, 5, 6, 7));
}
__device__ __forceinline__ unsigned pack_bf16(float a, float b) {
  return __builtin_bit_cast(unsigned, __builtin_convertvector(f32x2{a, b}, bf16x2));
}
__device__ __forceinline__ unsigned as_u(float v) { return __builtin_bit_cast(unsigned, v); }
__device__ __forceinline__ float as_f(unsigned v) { return __builtin_bit_cast(float, v); }
__device__ __forceinline__ float bf_lo(unsigned p) { return __builtin_bit_cast(float, p << 16); }
__device__ __forceinline__ float bf_hi(unsigned p) { return __builtin_bit_cast(float, p & 0xffff0000u); }
__device__ __forceinline__ float sigmoidf(float v) { return __builtin_amdgcn_rcpf(1.0f + __expf(-v)); }

__device__ __forceinline__ v4u tag_chunk(float a, float b, float c, float d, v4u tb) {
  return v4u{(__builtin_bit_cast(unsigned, a) & 0xffffff00u) | tb.x, (__builtin_bit_cast(unsigned, b) & 0xffffff00u) | tb.y,
             (__builtin_bit_cast(unsigned, c) & 0xffffff00u) | tb.z, (__builtin_bit_cast(unsigned, d) & 0xffffff00u) | tb.w};
}
template <int N, int STRIDE>
__device__ __forceinline__ f32x4 gather_sum(__amdgpu_buffer_rsrc_t rs, int off, v4u tb, bool& ok) {
  f32x4 sum;
#pragma nounroll
  for (unsigned spins = 0;; ++spins) {
    v4u c[N];
#pragma unroll
    for (int i = 0; i < N; ++i) c[i] = __builtin_amdgcn_raw_buffer_load_b128(rs, off, i * STRIDE, AUX_SC1);
    unsigned bad = 0;
#pragma unroll
    for (int i = 0; i < N; ++i) {
      c[i] ^= tb;
      bad |= c[i].x | c[i].y | c[i].z | c[i].w;
      sum = i ? sum + __builtin_bit_cast(f32x4, c[i]) : __builtin_bit_cast(f32x4, c[i]);
    }
    if (__builtin_amdgcn_ballot_w64((bad & 0xffu) != 0) == 0) break;
    if (spins > SPIN_CAP) { ok = false; break; }
    asm volatile("" ::: "memory");
  }
  return sum;
}

template <int I> __device__ __forceinline__ void stream_item(unsigned l1, unsigned v1, v4u r1, unsigned ld, unsigned vd0, unsigned vd1, v4u rd) {
  if constexpr (I < 32) dma_w<I * 1024, I * 8 * F * 2>(l1, v1, r1);
  else if constexpr (I < 40) dma_w<(I - 32) * 1024, (I - 32) * 8 * D * 2>(ld, (I & 1) ? vd1 : vd0, rd);
}
template <int LO, int HI> __device__ __forceinline__ void stream_range(unsigned l1, unsigned v1, v4u r1, unsigned ld, unsigned vd0, unsigned vd1, v4u rd) {
  if constexpr (LO < HI) { stream_item<LO>(l1, v1, r1, ld, vd0, vd1, rd); stream_range<LO + 1, HI>(l1, v1, r1, ld, vd0, vd1, rd); }
}
template <int SS> __device__ __forceinline__ void phase1_steps(unsigned l1, unsigned v1, v4u r1, unsigned ld, unsigned vd0, unsigned vd1, v4u rd,
                                                               LAS char* a0, LAS char* a1, const bf16x8 (&xf)[16], f32x16& c0, f32x16& c1) {
  if constexpr (SS < 16) {
    constexpr int issued = THR + 2 * SS < 40 ? THR + 2 * SS : 40;
    wait_vm<issued - (2 * SS + 2)>();
    stream_range<THR + 2 * SS, THR + 2 * SS + 2>(l1, v1, r1, ld, vd0, vd1, rd);
    if constexpr (THR + 2 * SS == 30) wg_barrier();
    c0 = __builtin_amdgcn_mfma_f32_32x32x16_bf16(tr_frag(a0 + SS * 2048), xf[SS], c0, 0, 0, 0);
    c1 = __builtin_amdgcn_mfma_f32_32x32x16_bf16(tr_frag(a1 + SS * 2048), xf[SS], c1, 0, 0, 0);
    phase1_steps<SS + 1>(l1, v1, r1, ld, vd0, vd1, rd, a0, a1, xf, c0, c1);
  }
}
template <int I> __device__ __forceinline__ void x_range(unsigned lx, unsigned vx, v4u rx) {
  if constexpr (I < 8) { dma_x<I * 1024, I * 2 * D * 2>(lx, vx ^ (I * 32), rx); x_range<I + 1>(lx, vx, rx); }
}
static_assert(THR % 2 == 0 && THR >= 8 && THR <= 30, "the step that issues the last gate/up piece holds the second barrier");

__device__ __forceinline__ float wave_sum(float v) {
#pragma unroll
  for (int o = 32; o; o >>= 1) v += __shfl_xor(v, o);
  return v;
}

__global__ void __launch_bounds__(256, 1)
mod_ffn_kernel(const u16* __restrict__ x, const u16* __restrict__ wr, const u16* __restrict__ wg, const u16* __restrict__ wu,
               const u16* __restrict__ wd, u16* __restrict__ out, char* ws) {
  __shared__ __attribute__((aligned(1024))) char smem_[L_BYTES];
  LAS char* smem = (LAS char*)smem_;
  const int lane = threadIdx.x & 63, wv = __builtin_amdgcn_readfirstlane(threadIdx.x >> 6);
  STAMP(0);
  {
    unsigned dep = 0; asm volatile("" : "+v"(dep));
#pragma unroll
    for (int i = 0; i < 1; ++i) {
      unsigned v = *(const GAS unsigned*)(ws + (32u << 20) + (size_t)i * 256u + dep);
      asm volatile("s_waitcnt vmcnt(0)\n\tv_and_b32 %0, 0, %1" : "=v"(dep) : "v"(v) : "memory");
    }
    asm volatile("" :: "v"(dep) : "memory");
  }
  unsigned tag = *(const CAS unsigned*)ws + 0x01010101u;

  if (blockIdx.x < NROUTER) {
    if (wv != 0) return;
    const int b = blockIdx.x;
    const GAS v4u* xp = (const GAS v4u*)(x + b * 4 * D) + lane;
    const GAS v4u* wp = (const GAS v4u*)wr + lane;
    v4u xv[4][2], wv2[2];
#pragma unroll
    for (int s = 0; s < 4; ++s) { xv[s][0] = xp[s * 128]; xv[s][1] = xp[s * 128 + 64]; }
    wv2[0] = wp[0]; wv2[1] = wp[64];
    int sel = 0; float best = 0.f;
#pragma unroll
    for (int s = 0; s < 4; ++s) {
      float a = 0.f;
#pragma unroll
      for (int i = 0; i < 2; ++i)
#pragma unroll
        for (int e = 0; e < 4; ++e) { a = fmaf(bf_lo(xv[s][i][e]), bf_lo(wv2[i][e]), a); a = fmaf(bf_hi(xv[s][i][e]), bf_hi(wv2[i][e]), a); }
      const float r = bf_lo(pack_bf16(wave_sum(a), 0.f));
      const bool take = s == 0 || r > best || (best != best && r == r);
      sel = take ? s : sel; best = take ? r : best;
    }
    STAMP(1);
    if (lane == 0)
      __hip_atomic_store((GAS u64*)(ws + WS_SEL) + b, ((u64)tag << 32) | (__builtin_bit_cast(unsigned, best) & 0xffff0000u) | (unsigned)sel, RLX_AGENT);
    GAS v4u* op = (GAS v4u*)(out + b * 4 * D) + lane;
#pragma unroll
    for (int s = 0; s < 4; ++s)
      if (s != sel) {
#pragma unroll
        for (int i = 0; i < 2; ++i) {
          v4u o;
#pragma unroll
          for (int e = 0; e < 4; ++e) { const float lo = bf_lo(xv[s][i][e]), hi = bf_hi(xv[s][i][e]); o[e] = pack_bf16(lo + lo * 0.0f, hi + hi * 0.0f); }
          op[s * 128 + i * 64] = o;
        }
      }
    STAMP_OUT();
    return;
  }

  const int wk = blockIdx.x - NROUTER, j = wk / NGROUP, g = wk - j * NGROUP;
  const unsigned lds0 = (unsigned)(size_t)smem;
  const v4u rd = srd(wd, W_BYTES);
  const unsigned sw = (lane & 7) ^ ((lane >> 2) & 4);
  const unsigned vd0 = (g * 64 + (lane >> 3)) * (D * 2) + j * 512 + wv * 128 + sw * 16, vd1 = vd0 ^ 32;
  const unsigned ld = lds0 + L_WD + wv * 8192;
  const __amdgpu_buffer_rsrc_t rw = __builtin_amdgcn_make_buffer_rsrc((void*)ws, 0, WS_BYTES, 0x00020000);
  const int q = (lane >> 2) & 3, p = lane & 3, G = lane >> 4;
  bool ok = true;
  v4u tb;

  if (wv < 2) {
    const v4u r1 = srd(wv ? wu : wg, W_BYTES);
    const unsigned v1 = (j * 256 + (lane >> 3)) * (F * 2) + g * 128 + sw * 16, l1 = lds0 + L_W1 + wv * 32768;
    stream_range<0, THR>(l1, v1, r1, ld, vd0, vd1, rd);
    STAMP(1);
    asm volatile("" : "+s"(tag));
    tb = v4u{tag & 255u, (tag >> 8) & 255u, (tag >> 16) & 255u, tag >> 24};
    wg_barrier();
    STAMP(2);
    bf16x8 xf[16];
    const unsigned xa = (lane & 31) * 512 + (((lane >> 5) ^ (lane & 15)) * 16);
#pragma unroll
    for (int s = 0; s < 16; ++s) xf[s] = *(LAS bf16x8*)(smem + L_X + (xa ^ (s * 32)));
    const unsigned ta = (8 * (G >> 1) + q) * 128 + ((32 * (G & 1) + 8 * p) ^ (64 * (q >> 1)));
    f32x16 c0 = {}, c1 = {};
    phase1_steps<0>(l1, v1, r1, ld, vd0, vd1, rd, smem + L_W1 + wv * 32768 + ta, smem + L_W1 + wv * 32768 + (ta ^ 64), xf, c0, c1);
    STAMP(3);
    const int b = (lane & 31) >> 2;
    if ((*(LAS unsigned*)(smem + L_SEL + b * 4) & 3u) == (unsigned)(lane & 3)) {
      const int off = WS_P1 + (g * 4 + j) * 4096 + wv * 2048 + (b * 2 + (lane >> 5)) * 16;
#pragma unroll
      for (int k = 0; k < 4; ++k) {
        __builtin_amdgcn_raw_buffer_store_b128(tag_chunk(c0[4 * k], c0[4 * k + 1], c0[4 * k + 2], c0[4 * k + 3], tb), rw, off + k * 256, 0, AUX_SC1);
        __builtin_amdgcn_raw_buffer_store_b128(tag_chunk(c1[4 * k], c1[4 * k + 1], c1[4 * k + 2], c1[4 * k + 3], tb), rw, off + 1024 + k * 256, 0, AUX_SC1);
      }
    }
    STAMP(4);
    wait_vm<8 + GREM>();
  } else {
    const v4u rx = srd(x, X_BYTES);
    const int t0 = (wv - 2) * 16 + (lane >> 5);
    x_range<0>(lds0 + L_X + (wv - 2) * 8192, t0 * (D * 2) + j * 512 + (((lane & 31) ^ (lane >> 5)) * 16), rx);
    if (lane < 32) *(LAS unsigned*)(smem + L_Z + lane * 4) = 0u;
    asm volatile("" : "+s"(tag));
    tb = v4u{tag & 255u, (tag >> 8) & 255u, (tag >> 16) & 255u, tag >> 24};
    wait_vm<0>();
    STAMP(1);
    wg_barrier();
    if (wv == 2) {
#pragma nounroll
      for (unsigned spins = 0;; ++spins) {
        const u64 v = __hip_atomic_load((GAS u64*)(ws + WS_SEL) + (lane & 7), RLX_AGENT);
        if (__builtin_amdgcn_ballot_w64((unsigned)(v >> 32) != tag) == 0) { if (lane < 8) *(LAS unsigned*)(smem + L_SEL + lane * 4) = (unsigned)v; break; }
        if (spins > SPIN_CAP) { ok = false; break; }
      }
    }
    STAMP(2);
    wg_barrier();
    STAMP(3);
    stream_range<32, 40>(0, 0, rd, ld, vd0, vd1, rd);
  }
  wg_barrier();
  STAMP(5);

  const int pos = wv * 32 + (lane & 31);
  const f32x4 s1 = gather_sum<4, 4096>(rw, WS_P1 + g * 16384 + (lane >> 5) * 2048 + pos * 16, tb, ok);
  STAMP(6);
  wait_vm<0>();
  bf16x8 wf[4][2];
  {
    const unsigned da = (8 * G + q) * 128 + ((8 * p) ^ (64 * (q >> 1)) ^ (32 * (G & 1)));
#pragma unroll
    for (int ct = 0; ct < 4; ++ct)
#pragma unroll
      for (int ks = 0; ks < 2; ++ks) wf[ct][ks] = tr_frag(smem + L_WD + wv * 8192 + (da ^ (32 * ct)) + ks * 4096);
  }
  {
    const v2u e = __builtin_amdgcn_permlane32_swap(as_u(s1[0]), as_u(s1[2]), false, false);
    const v2u o = __builtin_amdgcn_permlane32_swap(as_u(s1[1]), as_u(s1[3]), false, false);
    const unsigned gp = pack_bf16(as_f(e.x), as_f(o.x));
    const unsigned up = pack_bf16(as_f(e.y), as_f(o.y));
    const float g0 = bf_lo(gp), g1 = bf_hi(gp);
    const unsigned sp = pack_bf16(g0 * sigmoidf(g0), g1 * sigmoidf(g1));
    const unsigned hp = pack_bf16(bf_lo(sp) * bf_lo(up), bf_hi(sp) * bf_hi(up));
    const int col = (pos >> 4) * 8 + (pos & 1) * 4 + (lane >> 5) * 2;
    *(LAS unsigned*)(smem + L_H + ((pos >> 1) & 7) * H_ROW + col * 2) = hp;
  }
  wg_barrier();
  STAMP(7);

  {
    const unsigned ha = L_H + (lane & 7) * H_ROW + G * 16;
    LAS char* plo = smem + ((lane & 8) ? L_Z : ha);
    LAS char* phi = smem + ((lane & 8) ? ha : L_Z);
    const bf16x8 lo0 = *(LAS bf16x8*)plo, lo1 = *(LAS bf16x8*)(plo + 64), hi0 = *(LAS bf16x8*)phi, hi1 = *(LAS bf16x8*)(phi + 64);
    const int off = WS_P2 + g * 32768 + (j * 8 + wv * 2) * 1024 + (lane & 7) * 128 + (lane & 8) * 8 + G * 16;
#pragma unroll
    for (int a = 0; a < 2; ++a) {
      f32x4 c = {};
      c = __builtin_amdgcn_mfma_f32_16x16x32_bf16(wf[2 * a][0], lo0, c, 0, 0, 0);
      c = __builtin_amdgcn_mfma_f32_16x16x32_bf16(wf[2 * a][1], lo1, c, 0, 0, 0);
      c = __builtin_amdgcn_mfma_f32_16x16x32_bf16(wf[2 * a + 1][0], hi0, c, 0, 0, 0);
      c = __builtin_amdgcn_mfma_f32_16x16x32_bf16(wf[2 * a + 1][1], hi1, c, 0, 0, 0);
      __builtin_amdgcn_raw_buffer_store_b128(tag_chunk(c[0], c[1], c[2], c[3], tb), rw, off + a * 1024, 0, AUX_SC1);
    }
  }
  STAMP(8);

  if (wv == 2 || (wv == 3 && g < 8)) {
    const int t = g + (wv - 2) * NGROUP, m = t & 7, cb = t >> 3;
    const unsigned sv = *(LAS unsigned*)(smem + L_SEL + m * 4);
    const int tok = m * 4 + (sv & 3u);
    const int c8 = lane & 7;
    const v2u xr = *(LAS v2u*)(smem + L_X + tok * 512 + (((cb * 4 + (c8 >> 1)) ^ (tok & 15)) * 16) + (c8 & 1) * 8);
    const float rs = bf_lo(pack_bf16(sigmoidf(bf_hi(sv)), 0.f));
    f32x4 y = gather_sum<7, 8 * 32768>(rw, WS_P2 + (lane >> 3) * 32768 + (j * 8 + cb) * 1024 + m * 128 + c8 * 16, tb, ok);
    STAMP(9);
#pragma unroll
    for (int e = 0; e < 4; ++e) { y[e] += __shfl_xor(y[e], 8); y[e] += __shfl_xor(y[e], 16); y[e] += __shfl_xor(y[e], 32); }
    v2u o;
#pragma unroll
    for (int e = 0; e < 2; ++e) {
      const unsigned yp = pack_bf16(y[2 * e], y[2 * e + 1]);
      const unsigned pp = pack_bf16(bf_lo(yp) * rs, bf_hi(yp) * rs);
      const float x0 = bf_lo(xr[e]), x1 = bf_hi(xr[e]);
      o[e] = pack_bf16(x0 + (x0 * 0.0f + bf_lo(pp)), x1 + (x1 * 0.0f + bf_hi(pp)));
    }
    if (lane < 8) *(GAS v2u*)(out + tok * D + j * 256 + cb * 32 + c8 * 4) = o;
    if (wk == 0 && wv == 2 && lane == 0 && ok) __hip_atomic_store((GAS unsigned*)ws, tag, RLX_AGENT);
    STAMP(10);
  }
  STAMP_OUT();
}
}

extern "C" void kernel_launch(void* const* d_in, const int* in_sizes, int n_in, void* d_out, int out_size, void* d_ws, size_t ws_size,
                              hipStream_t stream) {
  mod_ffn_kernel<<<GRID, 256, 0, stream>>>((const u16*)d_in[0], (const u16*)d_in[1], (const u16*)d_in[2], (const u16*)d_in[3],
                                           (const u16*)d_in[4], (u16*)d_out, (char*)d_ws);
}
```

```cpp
#include <hip/hip_runtime.h>
#include <stdint.h>

#define WAUX " nt"
#define THR 12
#define GREM 4

namespace {
typedef unsigned short u16;
typedef unsigned long long u64;
typedef unsigned v4u __attribute__((ext_vector_type(4)));
typedef unsigned v2u __attribute__((ext_vector_type(2)));
typedef float f32x2 __attribute__((ext_vector_type(2)));
typedef float f32x4 __attribute__((ext_vector_type(4)));
typedef float f32x16 __attribute__((ext_vector_type(16)));
typedef short s16x4 __attribute__((ext_vector_type(4)));
typedef __bf16 bf16x2 __attribute__((ext_vector_type(2)));
typedef __bf16 bf16x8 __attribute__((ext_vector_type(8)));
#define GAS __attribute__((address_space(1)))
#define LAS __attribute__((address_space(3)))
#define CAS __attribute__((address_space(4)))
#define RLX_AGENT __ATOMIC_RELAXED, __HIP_MEMORY_SCOPE_AGENT

constexpr int D = 1024, F = 3584;
constexpr int NROUTER = 8, NGROUP = 56, GRID = NROUTER + 4 * NGROUP;
constexpr int W_BYTES = D * F * 2, X_BYTES = 32 * D * 2;
constexpr int WS_SEL = 1024, WS_P1 = 4096, WS_P2 = WS_P1 + 4 * NGROUP * 4096, WS_BYTES = WS_P2 + NGROUP * 32768;
constexpr int L_W1 = 0, L_WD = 65536, L_X = 98304, L_H = 114688, H_ROW = 144, L_Z = L_H + 8 * H_ROW, L_SEL = L_Z + 128,
              L_BYTES = L_SEL + 64;
constexpr int AUX_SC1 = 16;
constexpr unsigned SPIN_CAP = 1u << 16;

#define STAMP(i)
#define STAMP_OUT()

__device__ __forceinline__ v4u srd(const void* p, unsigned bytes) {
  const u64 a = (u64)p;
  return v4u{(unsigned)a, (unsigned)(a >> 32) & 0xffffu, bytes, 0x00020000u};
}
template <int LOFF, int GOFF> __device__ __forceinline__ void dma_w(unsigned lds, unsigned voff, v4u rs) {
  unsigned t;
  asm volatile("s_add_u32 m0, %1, %4\n\ts_mov_b32 %0, %5\n\tbuffer_load_dwordx4 %2, %3, %0 offen" WAUX " lds"
               : "=&s"(t) : "s"(lds), "v"(voff), "s"(rs), "i"(LOFF), "i"(GOFF) : "scc", "memory");
}
template <int LOFF, int GOFF> __device__ __forceinline__ void dma_x(unsigned lds, unsigned voff, v4u rs) {
  unsigned t;
  asm volatile("s_add_u32 m0, %1, %4\n\ts_mov_b32 %0, %5\n\tbuffer_load_dwordx4 %2, %3, %0 offen lds"
               : "=&s"(t) : "s"(lds), "v"(voff), "s"(rs), "i"(LOFF), "i"(GOFF) : "scc", "memory");
}
template <int N> __device__ __forceinline__ void wait_vm() { asm volatile("s_waitcnt vmcnt(%0)" ::"i"(N) : "memory"); }
__device__ __forceinline__ void wg_barrier() { asm volatile("s_waitcnt lgkmcnt(0)\n\ts_barrier" ::: "memory"); }

__device__ __forceinline__ bf16x8 tr_frag(LAS char* p) {
  const s16x4 lo = __builtin_amdgcn_ds_read_tr16_b64_v4i16((LAS s16x4*)p);
  const s16x4 hi = __builtin_amdgcn_ds_read_tr16_b64_v4i16((LAS s16x4*)(p + 512));
  return __builtin_bit_cast(bf16x8, __builtin_shufflevector(lo, hi, 0, 1, 2, 3, 4, 5, 6, 7));
}
__device__ __forceinline__ unsigned pack_bf16(float a, float b) {
  return __builtin_bit_cast(unsigned, __builtin_convertvector(f32x2{a, b}, bf16x2));
}
__device__ __forceinline__ unsigned as_u(float v) { return __builtin_bit_cast(unsigned, v); }
__device__ __forceinline__ float as_f(unsigned v) { return __builtin_bit_cast(float, v); }
__device__ __forceinline__ float bf_lo(unsigned p) { return __builtin_bit_cast(float, p << 16); }
__device__ __forceinline__ float bf_hi(unsigned p) { return __builtin_bit_cast(float, p & 0xffff0000u); }
__device__ __forceinline__ float sigmoidf(float v) { return __builtin_amdgcn_rcpf(1.0f + __expf(-v)); }

__device__ __forceinline__ v4u tag_chunk(float a, float b, float c, float d, v4u tb) {
  return v4u{(__builtin_bit_cast(unsigned, a) & 0xffffff00u) | tb.x, (__builtin_bit_cast(unsigned, b) & 0xffffff00u) | tb.y,
             (__builtin_bit_cast(unsigned, c) & 0xffffff00u) | tb.z, (__builtin_bit_cast(unsigned, d) & 0xffffff00u) | tb.w};
}
template <int N, int STRIDE>
__device__ __forceinline__ f32x4 gather_sum(__amdgpu_buffer_rsrc_t rs, int off, v4u tb, bool& ok) {
  f32x4 sum;
#pragma nounroll
  for (unsigned spins = 0;; ++spins) {
    v4u c[N];
#pragma unroll
    for (int i = 0; i < N; ++i) c[i] = __builtin_amdgcn_raw_buffer_load_b128(rs, off, i * STRIDE, AUX_SC1);
    unsigned bad = 0;
#pragma unroll
    for (int i = 0; i < N; ++i) {
      c[i] ^= tb;
      bad |= c[i].x | c[i].y | c[i].z | c[i].w;
      sum = i ? sum + __builtin_bit_cast(f32x4, c[i]) : __builtin_bit_cast(f32x4, c[i]);
    }
    if (__builtin_amdgcn_ballot_w64((bad & 0xffu) != 0) == 0) break;
    if (spins > SPIN_CAP) { ok = false; break; }
    asm volatile("" ::: "memory");
  }
  return sum;
}

template <int I> __device__ __forceinline__ void stream_item(unsigned l1, unsigned v1, v4u r1, unsigned ld, unsigned vd0, unsigned vd1, v4u rd) {
  if constexpr (I < 32) dma_w<I * 1024, I * 8 * F * 2>(l1, v1, r1);
  else if constexpr (I < 40) dma_w<(I - 32) * 1024, (I - 32) * 8 * D * 2>(ld, (I & 1) ? vd1 : vd0, rd);
}
template <int LO, int HI> __device__ __forceinline__ void stream_range(unsigned l1, unsigned v1, v4u r1, unsigned ld, unsigned vd0, unsigned vd1, v4u rd) {
  if constexpr (LO < HI) { stream_item<LO>(l1, v1, r1, ld, vd0, vd1, rd); stream_range<LO + 1, HI>(l1, v1, r1, ld, vd0, vd1, rd); }
}
template <int SS> __device__ __forceinline__ void phase1_steps(unsigned l1, unsigned v1, v4u r1, unsigned ld, unsigned vd0, unsigned vd1, v4u rd,
                                                               LAS char* a0, LAS char* a1, const bf16x8 (&xf)[16], f32x16& c0, f32x16& c1) {
  if constexpr (SS < 16) {
    constexpr int issued = THR + 2 * SS < 40 ? THR + 2 * SS : 40;
    wait_vm<issued - (2 * SS + 2)>();
    stream_range<THR + 2 * SS, THR + 2 * SS + 2>(l1, v1, r1, ld, vd0, vd1, rd);
    if constexpr (THR + 2 * SS == 30) wg_barrier();
    c0 = __builtin_amdgcn_mfma_f32_32x32x16_bf16(tr_frag(a0 + SS * 2048), xf[SS], c0, 0, 0, 0);
    c1 = __builtin_amdgcn_mfma_f32_32x32x16_bf16(tr_frag(a1 + SS * 2048), xf[SS], c1, 0, 0, 0);
    phase1_steps<SS + 1>(l1, v1, r1, ld, vd0, vd1, rd, a0, a1, xf, c0, c1);
  }
}
template <int I> __device__ __forceinline__ void x_range(unsigned lx, unsigned vx, v4u rx) {
  if constexpr (I < 8) { dma_x<I * 1024, I * 2 * D * 2>(lx, vx ^ (I * 32), rx); x_range<I + 1>(lx, vx, rx); }
}
static_assert(THR % 2 == 0 && THR >= 8 && THR <= 30, "the step that issues the last gate/up piece holds the second barrier");

__device__ __forceinline__ float wave_sum(float v) {
#pragma unroll
  for (int o = 32; o; o >>= 1) v += __shfl_xor(v, o);
  return v;
}

__global__ void __launch_bounds__(256, 1)
mod_ffn_kernel(const u16* __restrict__ x, const u16* __restrict__ wr, const u16* __restrict__ wg, const u16* __restrict__ wu,
               const u16* __restrict__ wd, u16* __restrict__ out, char* ws) {
  __shared__ __attribute__((aligned(1024))) char smem_[L_BYTES];
  LAS char* smem = (LAS char*)smem_;
  const int lane = threadIdx.x & 63, wv = __builtin_amdgcn_readfirstlane(threadIdx.x >> 6);
  STAMP(0);
  u64 tstart; asm volatile("s_memrealtime %0" : "=s"(tstart) :: "memory");
  unsigned tag = *(const CAS unsigned*)ws + 0x01010101u;

  if (blockIdx.x < NROUTER) {
    if (wv != 0) return;
    const int b = blockIdx.x;
    const GAS v4u* xp = (const GAS v4u*)(x + b * 4 * D) + lane;
    const GAS v4u* wp = (const GAS v4u*)wr + lane;
    v4u xv[4][2], wv2[2];
#pragma unroll
    for (int s = 0; s < 4; ++s) { xv[s][0] = xp[s * 128]; xv[s][1] = xp[s * 128 + 64]; }
    wv2[0] = wp[0]; wv2[1] = wp[64];
    int sel = 0; float best = 0.f;
#pragma unroll
    for (int s = 0; s < 4; ++s) {
      float a = 0.f;
#pragma unroll
      for (int i = 0; i < 2; ++i)
#pragma unroll
        for (int e = 0; e < 4; ++e) { a = fmaf(bf_lo(xv[s][i][e]), bf_lo(wv2[i][e]), a); a = fmaf(bf_hi(xv[s][i][e]), bf_hi(wv2[i][e]), a); }
      const float r = bf_lo(pack_bf16(wave_sum(a), 0.f));
      const bool take = s == 0 || r > best || (best != best && r == r);
      sel = take ? s : sel; best = take ? r : best;
    }
    STAMP(1);
    if (lane == 0)
      __hip_atomic_store((GAS u64*)(ws + WS_SEL) + b, ((u64)tag << 32) | (__builtin_bit_cast(unsigned, best) & 0xffff0000u) | (unsigned)sel, RLX_AGENT);
    GAS v4u* op = (GAS v4u*)(out + b * 4 * D) + lane;
#pragma unroll
    for (int s = 0; s < 4; ++s)
      if (s != sel) {
#pragma unroll
        for (int i = 0; i < 2; ++i) {
          v4u o;
#pragma unroll
          for (int e = 0; e < 4; ++e) { const float lo = bf_lo(xv[s][i][e]), hi = bf_hi(xv[s][i][e]); o[e] = pack_bf16(lo + lo * 0.0f, hi + hi * 0.0f); }
          op[s * 128 + i * 64] = o;
        }
      }
    STAMP_OUT();
    return;
  }

  const int wk = blockIdx.x - NROUTER, j = wk / NGROUP, g = wk - j * NGROUP;
  const unsigned lds0 = (unsigned)(size_t)smem;
  const v4u rd = srd(wd, W_BYTES);
  const unsigned sw = (lane & 7) ^ ((lane >> 2) & 4);
  const unsigned vd0 = (g * 64 + (lane >> 3)) * (D * 2) + j * 512 + wv * 128 + sw * 16, vd1 = vd0 ^ 32;
  const unsigned ld = lds0 + L_WD + wv * 8192;
  const __amdgpu_buffer_rsrc_t rw = __builtin_amdgcn_make_buffer_rsrc((void*)ws, 0, WS_BYTES, 0x00020000);
  const int q = (lane >> 2) & 3, p = lane & 3, G = lane >> 4;
  bool ok = true;
  v4u tb;

  if (wv < 2) {
    const v4u r1 = srd(wv ? wu : wg, W_BYTES);
    const unsigned v1 = (j * 256 + (lane >> 3)) * (F * 2) + g * 128 + sw * 16, l1 = lds0 + L_W1 + wv * 32768;
    stream_range<0, THR>(l1, v1, r1, ld, vd0, vd1, rd);
    STAMP(1);
    asm volatile("" : "+s"(tag));
    tb = v4u{tag & 255u, (tag >> 8) & 255u, (tag >> 16) & 255u, tag >> 24};
    wg_barrier();
    STAMP(2);
    bf16x8 xf[16];
    const unsigned xa = (lane & 31) * 512 + (((lane >> 5) ^ (lane & 15)) * 16);
#pragma unroll
    for (int s = 0; s < 16; ++s) xf[s] = *(LAS bf16x8*)(smem + L_X + (xa ^ (s * 32)));
    const unsigned ta = (8 * (G >> 1) + q) * 128 + ((32 * (G & 1) + 8 * p) ^ (64 * (q >> 1)));
    f32x16 c0 = {}, c1 = {};
    phase1_steps<0>(l1, v1, r1, ld, vd0, vd1, rd, smem + L_W1 + wv * 32768 + ta, smem + L_W1 + wv * 32768 + (ta ^ 64), xf, c0, c1);
    STAMP(3);
    const int b = (lane & 31) >> 2;
    if ((*(LAS unsigned*)(smem + L_SEL + b * 4) & 3u) == (unsigned)(lane & 3)) {
      const int off = WS_P1 + (g * 4 + j) * 4096 + wv * 2048 + (b * 2 + (lane >> 5)) * 16;
#pragma unroll
      for (int k = 0; k < 4; ++k) {
        __builtin_amdgcn_raw_buffer_store_b128(tag_chunk(c0[4 * k], c0[4 * k + 1], c0[4 * k + 2], c0[4 * k + 3], tb), rw, off + k * 256, 0, AUX_SC1);
        __builtin_amdgcn_raw_buffer_store_b128(tag_chunk(c1[4 * k], c1[4 * k + 1], c1[4 * k + 2], c1[4 * k + 3], tb), rw, off + 1024 + k * 256, 0, AUX_SC1);
      }
    }
    STAMP(4);
    wait_vm<8 + GREM>();
  } else {
    const v4u rx = srd(x, X_BYTES);
    const int t0 = (wv - 2) * 16 + (lane >> 5);
    x_range<0>(lds0 + L_X + (wv - 2) * 8192, t0 * (D * 2) + j * 512 + (((lane & 31) ^ (lane >> 5)) * 16), rx);
    if (lane < 32) *(LAS unsigned*)(smem + L_Z + lane * 4) = 0u;
    asm volatile("" : "+s"(tag));
    tb = v4u{tag & 255u, (tag >> 8) & 255u, (tag >> 16) & 255u, tag >> 24};
    wait_vm<0>();
    STAMP(1);
    wg_barrier();
    if (wv == 2) {
#pragma nounroll
      for (unsigned spins = 0;; ++spins) {
        const u64 v = __hip_atomic_load((GAS u64*)(ws + WS_SEL) + (lane & 7), RLX_AGENT);
        if (__builtin_amdgcn_ballot_w64((unsigned)(v >> 32) != tag) == 0) { if (lane < 8) *(LAS unsigned*)(smem + L_SEL + lane * 4) = (unsigned)v; break; }
        if (spins > SPIN_CAP) { ok = false; break; }
      }
    }
    STAMP(2);
    wg_barrier();
    STAMP(3);
    stream_range<32, 40>(0, 0, rd, ld, vd0, vd1, rd);
  }
  wg_barrier();
  STAMP(5);

  const int pos = wv * 32 + (lane & 31);
  const f32x4 s1 = gather_sum<4, 4096>(rw, WS_P1 + g * 16384 + (lane >> 5) * 2048 + pos * 16, tb, ok);
  STAMP(6);
  wait_vm<0>();
  bf16x8 wf[4][2];
  {
    const unsigned da = (8 * G + q) * 128 + ((8 * p) ^ (64 * (q >> 1)) ^ (32 * (G & 1)));
#pragma unroll
    for (int ct = 0; ct < 4; ++ct)
#pragma unroll
      for (int ks = 0; ks < 2; ++ks) wf[ct][ks] = tr_frag(smem + L_WD + wv * 8192 + (da ^ (32 * ct)) + ks * 4096);
  }
  {
    const v2u e = __builtin_amdgcn_permlane32_swap(as_u(s1[0]), as_u(s1[2]), false, false);
    const v2u o = __builtin_amdgcn_permlane32_swap(as_u(s1[1]), as_u(s1[3]), false, false);
    const unsigned gp = pack_bf16(as_f(e.x), as_f(o.x));
    const unsigned up = pack_bf16(as_f(e.y), as_f(o.y));
    const float g0 = bf_lo(gp), g1 = bf_hi(gp);
    const unsigned sp = pack_bf16(g0 * sigmoidf(g0), g1 * sigmoidf(g1));
    const unsigned hp = pack_bf16(bf_lo(sp) * bf_lo(up), bf_hi(sp) * bf_hi(up));
    const int col = (pos >> 4) * 8 + (pos & 1) * 4 + (lane >> 5) * 2;
    *(LAS unsigned*)(smem + L_H + ((pos >> 1) & 7) * H_ROW + col * 2) = hp;
  }
  wg_barrier();
  STAMP(7);

  {
    const unsigned ha = L_H + (lane & 7) * H_ROW + G * 16;
    LAS char* plo = smem + ((lane & 8) ? L_Z : ha);
    LAS char* phi = smem + ((lane & 8) ? ha : L_Z);
    const bf16x8 lo0 = *(LAS bf16x8*)plo, lo1 = *(LAS bf16x8*)(plo + 64), hi0 = *(LAS bf16x8*)phi, hi1 = *(LAS bf16x8*)(phi + 64);
    const int off = WS_P2 + g * 32768 + (j * 8 + wv * 2) * 1024 + (lane & 7) * 128 + (lane & 8) * 8 + G * 16;
#pragma unroll
    for (int a = 0; a < 2; ++a) {
      f32x4 c = {};
      c = __builtin_amdgcn_mfma_f32_16x16x32_bf16(wf[2 * a][0], lo0, c, 0, 0, 0);
      c = __builtin_amdgcn_mfma_f32_16x16x32_bf16(wf[2 * a][1], lo1, c, 0, 0, 0);
      c = __builtin_amdgcn_mfma_f32_16x16x32_bf16(wf[2 * a + 1][0], hi0, c, 0, 0, 0);
      c = __builtin_amdgcn_mfma_f32_16x16x32_bf16(wf[2 * a + 1][1], hi1, c, 0, 0, 0);
      __builtin_amdgcn_raw_buffer_store_b128(tag_chunk(c[0], c[1], c[2], c[3], tb), rw, off + a * 1024, 0, AUX_SC1);
    }
  }
  STAMP(8);

  if (wv == 2 || (wv == 3 && g < 8)) {
    const int t = g + (wv - 2) * NGROUP, m = t & 7, cb = t >> 3;
    const unsigned sv = *(LAS unsigned*)(smem + L_SEL + m * 4);
    const int tok = m * 4 + (sv & 3u);
    const int c8 = lane & 7;
    const v2u xr = *(LAS v2u*)(smem + L_X + tok * 512 + (((cb * 4 + (c8 >> 1)) ^ (tok & 15)) * 16) + (c8 & 1) * 8);
    const float rs = bf_lo(pack_bf16(sigmoidf(bf_hi(sv)), 0.f));
    f32x4 y = gather_sum<7, 8 * 32768>(rw, WS_P2 + (lane >> 3) * 32768 + (j * 8 + cb) * 1024 + m * 128 + c8 * 16, tb, ok);
    STAMP(9);
#pragma unroll
    for (int e = 0; e < 4; ++e) { y[e] += __shfl_xor(y[e], 8); y[e] += __shfl_xor(y[e], 16); y[e] += __shfl_xor(y[e], 32); }
    v2u o;
#pragma unroll
    for (int e = 0; e < 2; ++e) {
      const unsigned yp = pack_bf16(y[2 * e], y[2 * e + 1]);
      const unsigned pp = pack_bf16(bf_lo(yp) * rs, bf_hi(yp) * rs);
      const float x0 = bf_lo(xr[e]), x1 = bf_hi(xr[e]);
      o[e] = pack_bf16(x0 + (x0 * 0.0f + bf_lo(pp)), x1 + (x1 * 0.0f + bf_hi(pp)));
    }
    if (lane < 8) *(GAS v2u*)(out + tok * D + j * 256 + cb * 32 + c8 * 4) = o;
    if (wk == 0 && wv == 2 && lane == 0 && ok) __hip_atomic_store((GAS unsigned*)ws, tag, RLX_AGENT);
    STAMP(10);
  }
  if (wv == 2) { asm volatile("s_waitcnt lgkmcnt(0)" : "+s"(tstart)); while (__builtin_amdgcn_s_memrealtime() - tstart < 800ull) __builtin_amdgcn_s_sleep(1); }
  STAMP_OUT();
}
}

extern "C" void kernel_launch(void* const* d_in, const int* in_sizes, int n_in, void* d_out, int out_size, void* d_ws, size_t ws_size,
                              hipStream_t stream) {
  mod_ffn_kernel<<<GRID, 256, 0, stream>>>((const u16*)d_in[0], (const u16*)d_in[1], (const u16*)d_in[2], (const u16*)d_in[3],
                                           (const u16*)d_in[4], (u16*)d_out, (char*)d_ws);
}
```

```cpp
#include <hip/hip_runtime.h>
#include <stdint.h>

#define WAUX " nt"
#define THR 12
#define GREM 4

namespace {
typedef unsigned short u16;
typedef unsigned long long u64;
typedef unsigned v4u __attribute__((ext_vector_type(4)));
typedef unsigned v2u __attribute__((ext_vector_type(2)));
typedef float f32x2 __attribute__((ext_vector_type(2)));
typedef float f32x4 __attribute__((ext_vector_type(4)));
typedef float f32x16 __attribute__((ext_vector_type(16)));
typedef short s16x4 __attribute__((ext_vector_type(4)));
typedef __bf16 bf16x2 __attribute__((ext_vector_type(2)));
typedef __bf16 bf16x8 __attribute__((ext_vector_type(8)));
#define GAS __attribute__((address_space(1)))
#define LAS __attribute__((address_space(3)))
#define CAS __attribute__((address_space(4)))
#define RLX_AGENT __ATOMIC_RELAXED, __HIP_MEMORY_SCOPE_AGENT

constexpr int D = 1024, F = 3584;
constexpr int NROUTER = 8, NGROUP = 56, GRID = NROUTER + 4 * NGROUP;
constexpr int W_BYTES = D * F * 2, X_BYTES = 32 * D * 2;
constexpr int WS_SEL = 1024, WS_P1 = 4096, WS_P2 = WS_P1 + 4 * NGROUP * 4096, WS_BYTES = WS_P2 + NGROUP * 32768;
constexpr int L_W1 = 0, L_WD = 65536, L_X = 98304, L_H = 114688, H_ROW = 144, L_Z = L_H + 8 * H_ROW, L_SEL = L_Z + 128,
              L_BYTES = L_SEL + 64;
constexpr int AUX_SC1 = 16;
constexpr unsigned SPIN_CAP = 1u << 16;

#define STAMP(i)
#define STAMP_OUT()

__device__ __forceinline__ v4u srd(const void* p, unsigned bytes) {
  const u64 a = (u64)p;
  return v4u{(unsigned)a, (unsigned)(a >> 32) & 0xffffu, bytes, 0x00020000u};
}
template <int LOFF, int GOFF> __device__ __forceinline__ void dma_w(unsigned lds, unsigned voff, v4u rs) {
  unsigned t;
  asm volatile("s_add_u32 m0, %1, %4\n\ts_mov_b32 %0, %5\n\tbuffer_load_dwordx4 %2, %3, %0 offen" WAUX " lds"
               : "=&s"(t) : "s"(lds), "v"(voff), "s"(rs), "i"(LOFF), "i"(GOFF) : "scc", "memory");
}
template <int LOFF, int GOFF> __device__ __forceinline__ void dma_x(unsigned lds, unsigned voff, v4u rs) {
  unsigned t;
  asm volatile("s_add_u32 m0, %1, %4\n\ts_mov_b32 %0, %5\n\tbuffer_load_dwordx4 %2, %3, %0 offen lds"
               : "=&s"(t) : "s"(lds), "v"(voff), "s"(rs), "i"(LOFF), "i"(GOFF) : "scc", "memory");
}
template <int N> __device__ __forceinline__ void wait_vm() { asm volatile("s_waitcnt vmcnt(%0)" ::"i"(N) : "memory"); }
__device__ __forceinline__ void wg_barrier() { asm volatile("s_waitcnt lgkmcnt(0)\n\ts_barrier" ::: "memory"); }

__device__ __forceinline__ bf16x8 tr_frag(LAS char* p) {
  const s16x4 lo = __builtin_amdgcn_ds_read_tr16_b64_v4i16((LAS s16x4*)p);
  const s16x4 hi = __builtin_amdgcn_ds_read_tr16_b64_v4i16((LAS s16x4*)(p + 512));
  return __builtin_bit_cast(bf16x8, __builtin_shufflevector(lo, hi, 0, 1, 2, 3, 4, 5, 6, 7));
}
__device__ __forceinline__ unsigned pack_bf16(float a, float b) {
  return __builtin_bit_cast(unsigned, __builtin_convertvector(f32x2{a, b}, bf16x2));
}
__device__ __forceinline__ unsigned as_u(float v) { return __builtin_bit_cast(unsigned, v); }
__device__ __forceinline__ float as_f(unsigned v) { return __builtin_bit_cast(float, v); }
__device__ __forceinline__ float bf_lo(unsigned p) { return __builtin_bit_cast(float, p << 16); }
__device__ __forceinline__ float bf_hi(unsigned p) { return __builtin_bit_cast(float, p & 0xffff0000u); }
__device__ __forceinline__ float sigmoidf(float v) { return __builtin_amdgcn_rcpf(1.0f + __expf(-v)); }

__device__ __forceinline__ v4u tag_chunk(float a, float b, float c, float d, v4u tb) {
  return v4u{(__builtin_bit_cast(unsigned, a) & 0xffffff00u) | tb.x, (__builtin_bit_cast(unsigned, b) & 0xffffff00u) | tb.y,
             (__builtin_bit_cast(unsigned, c) & 0xffffff00u) | tb.z, (__builtin_bit_cast(unsigned, d) & 0xffffff00u) | tb.w};
}
template <int N, int STRIDE>
__device__ __forceinline__ f32x4 gather_sum(__amdgpu_buffer_rsrc_t rs, int off, v4u tb, bool& ok) {
  f32x4 sum;
#pragma nounroll
  for (unsigned spins = 0;; ++spins) {
    v4u c[N];
#pragma unroll
    for (int i = 0; i < N; ++i) c[i] = __builtin_amdgcn_raw_buffer_load_b128(rs, off, i * STRIDE, AUX_SC1);
    unsigned bad = 0;
#pragma unroll
    for (int i = 0; i < N; ++i) {
      c[i] ^= tb;
      bad |= c[i].x | c[i].y | c[i].z | c[i].w;
      sum = i ? sum + __builtin_bit_cast(f32x4, c[i]) : __builtin_bit_cast(f32x4, c[i]);
    }
    if (__builtin_amdgcn_ballot_w64((bad & 0xffu) != 0) == 0) break;
    if (spins > SPIN_CAP) { ok = false; break; }
    asm volatile("" ::: "memory");
  }
  return sum;
}

template <int I> __device__ __forceinline__ void stream_item(unsigned l1, unsigned v1, v4u r1, unsigned ld, unsigned vd0, unsigned vd1, v4u rd) {
  if constexpr (I < 32) dma_w<I * 1024, I * 8 * F * 2>(l1, v1, r1);
  else if constexpr (I < 40) dma_w<(I - 32) * 1024, (I - 32) * 8 * D * 2>(ld, (I & 1) ? vd1 : vd0, rd);
}
template <int LO, int HI> __device__ __forceinline__ void stream_range(unsigned l1, unsigned v1, v4u r1, unsigned ld, unsigned vd0, unsigned vd1, v4u rd) {
  if constexpr (LO < HI) { stream_item<LO>(l1, v1, r1, ld, vd0, vd1, rd); stream_range<LO + 1, HI>(l1, v1, r1, ld, vd0, vd1, rd); }
}
template <int SS> __device__ __forceinline__ void phase1_steps(unsigned l1, unsigned v1, v4u r1, unsigned ld, unsigned vd0, unsigned vd1, v4u rd,
                                                               LAS char* a0, LAS char* a1, const bf16x8 (&xf)[16], f32x16& c0, f32x16& c1) {
  if constexpr (SS < 16) {
    constexpr int issued = THR + 2 * SS < 40 ? THR + 2 * SS : 40;
    wait_vm<issued - (2 * SS + 2)>();
    stream_range<THR + 2 * SS, THR + 2 * SS + 2>(l1, v1, r1, ld, vd0, vd1, rd);
    if constexpr (THR + 2 * SS == 30) wg_barrier();
    c0 = __builtin_amdgcn_mfma_f32_32x32x16_bf16(tr_frag(a0 + SS * 2048), xf[SS], c0, 0, 0, 0);
    c1 = __builtin_amdgcn_mfma_f32_32x32x16_bf16(tr_frag(a1 + SS * 2048), xf[SS], c1, 0, 0, 0);
    phase1_steps<SS + 1>(l1, v1, r1, ld, vd0, vd1, rd, a0, a1, xf, c0, c1);
  }
}
template <int I> __device__ __forceinline__ void x_range(unsigned lx, unsigned vx, v4u rx) {
  if constexpr (I < 8) { dma_x<I * 1024, I * 2 * D * 2>(lx, vx ^ (I * 32), rx); x_range<I + 1>(lx, vx, rx); }
}
static_assert(THR % 2 == 0 && THR >= 8 && THR <= 30, "the step that issues the last gate/up piece holds the second barrier");

__device__ __forceinline__ float wave_sum(float v) {
#pragma unroll
  for (int o = 32; o; o >>= 1) v += __shfl_xor(v, o);
  return v;
}

__global__ void __launch_bounds__(1024, 1)
mod_ffn_kernel(const u16* __restrict__ x, const u16* __restrict__ wr, const u16* __restrict__ wg, const u16* __restrict__ wu,
               const u16* __restrict__ wd, u16* __restrict__ out, char* ws) {
  __shared__ __attribute__((aligned(1024))) char smem_[L_BYTES];
  LAS char* smem = (LAS char*)smem_;
  const int lane = threadIdx.x & 63, wv = __builtin_amdgcn_readfirstlane(threadIdx.x >> 6);
  STAMP(0);
  u64 tstart; asm volatile("s_memrealtime %0" : "=s"(tstart) :: "memory");
  unsigned tag = *(const CAS unsigned*)ws + 0x01010101u;

  if (wv >= 4) return;
  if (blockIdx.x < NROUTER) {
    if (wv != 0) return;
    const int b = blockIdx.x;
    const GAS v4u* xp = (const GAS v4u*)(x + b * 4 * D) + lane;
    const GAS v4u* wp = (const GAS v4u*)wr + lane;
    v4u xv[4][2], wv2[2];
#pragma unroll
    for (int s = 0; s < 4; ++s) { xv[s][0] = xp[s * 128]; xv[s][1] = xp[s * 128 + 64]; }
    wv2[0] = wp[0]; wv2[1] = wp[64];
    int sel = 0; float best = 0.f;
#pragma unroll
    for (int s = 0; s < 4; ++s) {
      float a = 0.f;
#pragma unroll
      for (int i = 0; i < 2; ++i)
#pragma unroll
        for (int e = 0; e < 4; ++e) { a = fmaf(bf_lo(xv[s][i][e]), bf_lo(wv2[i][e]), a); a = fmaf(bf_hi(xv[s][i][e]), bf_hi(wv2[i][e]), a); }
      const float r = bf_lo(pack_bf16(wave_sum(a), 0.f));
      const bool take = s == 0 || r > best || (best != best && r == r);
      sel = take ? s : sel; best = take ? r : best;
    }
    STAMP(1);
    if (lane == 0)
      __hip_atomic_store((GAS u64*)(ws + WS_SEL) + b, ((u64)tag << 32) | (__builtin_bit_cast(unsigned, best) & 0xffff0000u) | (unsigned)sel, RLX_AGENT);
    GAS v4u* op = (GAS v4u*)(out + b * 4 * D) + lane;
#pragma unroll
    for (int s = 0; s < 4; ++s)
      if (s != sel) {
#pragma unroll
        for (int i = 0; i < 2; ++i) {
          v4u o;
#pragma unroll
          for (int e = 0; e < 4; ++e) { const float lo = bf_lo(xv[s][i][e]), hi = bf_hi(xv[s][i][e]); o[e] = pack_bf16(lo + lo * 0.0f, hi + hi * 0.0f); }
          op[s * 128 + i * 64] = o;
        }
      }
    STAMP_OUT();
    return;
  }

  const int wk = blockIdx.x - NROUTER, j = wk / NGROUP, g = wk - j * NGROUP;
  const unsigned lds0 = (unsigned)(size_t)smem;
  const v4u rd = srd(wd, W_BYTES);
  const unsigned sw = (lane & 7) ^ ((lane >> 2) & 4);
  const unsigned vd0 = (g * 64 + (lane >> 3)) * (D * 2) + j * 512 + wv * 128 + sw * 16, vd1 = vd0 ^ 32;
  const unsigned ld = lds0 + L_WD + wv * 8192;
  const __amdgpu_buffer_rsrc_t rw = __builtin_amdgcn_make_buffer_rsrc((void*)ws, 0, WS_BYTES, 0x00020000);
  const int q = (lane >> 2) & 3, p = lane & 3, G = lane >> 4;
  bool ok = true;
  v4u tb;

  if (wv < 2) {
    const v4u r1 = srd(wv ? wu : wg, W_BYTES);
    const unsigned v1 = (j * 256 + (lane >> 3)) * (F * 2) + g * 128 + sw * 16, l1 = lds0 + L_W1 + wv * 32768;
    stream_range<0, THR>(l1, v1, r1, ld, vd0, vd1, rd);
    STAMP(1);
    asm volatile("" : "+s"(tag));
    tb = v4u{tag & 255u, (tag >> 8) & 255u, (tag >> 16) & 255u, tag >> 24};
    wg_barrier();
    STAMP(2);
    bf16x8 xf[16];
    const unsigned xa = (lane & 31) * 512 + (((lane >> 5) ^ (lane & 15)) * 16);
#pragma unroll
    for (int s = 0; s < 16; ++s) xf[s] = *(LAS bf16x8*)(smem + L_X + (xa ^ (s * 32)));
    const unsigned ta = (8 * (G >> 1) + q) * 128 + ((32 * (G & 1) + 8 * p) ^ (64 * (q >> 1)));
    f32x16 c0 = {}, c1 = {};
    phase1_steps<0>(l1, v1, r1, ld, vd0, vd1, rd, smem + L_W1 + wv * 32768 + ta, smem + L_W1 + wv * 32768 + (ta ^ 64), xf, c0, c1);
    STAMP(3);
    const int b = (lane & 31) >> 2;
    if ((*(LAS unsigned*)(smem + L_SEL + b * 4) & 3u) == (unsigned)(lane & 3)) {
      const int off = WS_P1 + (g * 4 + j) * 4096 + wv * 2048 + (b * 2 + (lane >> 5)) * 16;
#pragma unroll
      for (int k = 0; k < 4; ++k) {
        __builtin_amdgcn_raw_buffer_store_b128(tag_chunk(c0[4 * k], c0[4 * k + 1], c0[4 * k + 2], c0[4 * k + 3], tb), rw, off + k * 256, 0, AUX_SC1);
        __builtin_amdgcn_raw_buffer_store_b128(tag_chunk(c1[4 * k], c1[4 * k + 1], c1[4 * k + 2], c1[4 * k + 3], tb), rw, off + 1024 + k * 256, 0, AUX_SC1);
      }
    }
    STAMP(4);
    wait_vm<8 + GREM>();
  } else {
    const v4u rx = srd(x, X_BYTES);
    const int t0 = (wv - 2) * 16 + (lane >> 5);
    x_range<0>(lds0 + L_X + (wv - 2) * 8192, t0 * (D * 2) + j * 512 + (((lane & 31) ^ (lane >> 5)) * 16), rx);
    if (lane < 32) *(LAS unsigned*)(smem + L_Z + lane * 4) = 0u;
    asm volatile("" : "+s"(tag));
    tb = v4u{tag & 255u, (tag >> 8) & 255u, (tag >> 16) & 255u, tag >> 24};
    wait_vm<0>();
    STAMP(1);
    wg_barrier();
    if (wv == 2) {
#pragma nounroll
      for (unsigned spins = 0;; ++spins) {
        const u64 v = __hip_atomic_load((GAS u64*)(ws + WS_SEL) + (lane & 7), RLX_AGENT);
        if (__builtin_amdgcn_ballot_w64((unsigned)(v >> 32) != tag) == 0) { if (lane < 8) *(LAS unsigned*)(smem + L_SEL + lane * 4) = (unsigned)v; break; }
        if (spins > SPIN_CAP) { ok = false; break; }
      }
    }
    STAMP(2);
    wg_barrier();
    STAMP(3);
    stream_range<32, 40>(0, 0, rd, ld, vd0, vd1, rd);
  }
  wg_barrier();
  STAMP(5);

  const int pos = wv * 32 + (lane & 31);
  const f32x4 s1 = gather_sum<4, 4096>(rw, WS_P1 + g * 16384 + (lane >> 5) * 2048 + pos * 16, tb, ok);
  STAMP(6);
  wait_vm<0>();
  bf16x8 wf[4][2];
  {
    const unsigned da = (8 * G + q) * 128 + ((8 * p) ^ (64 * (q >> 1)) ^ (32 * (G & 1)));
#pragma unroll
    for (int ct = 0; ct < 4; ++ct)
#pragma unroll
      for (int ks = 0; ks < 2; ++ks) wf[ct][ks] = tr_frag(smem + L_WD + wv * 8192 + (da ^ (32 * ct)) + ks * 4096);
  }
  {
    const v2u e = __builtin_amdgcn_permlane32_swap(as_u(s1[0]), as_u(s1[2]), false, false);
    const v2u o = __builtin_amdgcn_permlane32_swap(as_u(s1[1]), as_u(s1[3]), false, false);
    const unsigned gp = pack_bf16(as_f(e.x), as_f(o.x));
    const unsigned up = pack_bf16(as_f(e.y), as_f(o.y));
    const float g0 = bf_lo(gp), g1 = bf_hi(gp);
    const unsigned sp = pack_bf16(g0 * sigmoidf(g0), g1 * sigmoidf(g1));
    const unsigned hp = pack_bf16(bf_lo(sp) * bf_lo(up), bf_hi(sp) * bf_hi(up));
    const int col = (pos >> 4) * 8 + (pos & 1) * 4 + (lane >> 5) * 2;
    *(LAS unsigned*)(smem + L_H + ((pos >> 1) & 7) * H_ROW + col * 2) = hp;
  }
  wg_barrier();
  STAMP(7);

  {
    const unsigned ha = L_H + (lane & 7) * H_ROW + G * 16;
    LAS char* plo = smem + ((lane & 8) ? L_Z : ha);
    LAS char* phi = smem + ((lane & 8) ? ha : L_Z);
    const bf16x8 lo0 = *(LAS bf16x8*)plo, lo1 = *(LAS bf16x8*)(plo + 64), hi0 = *(LAS bf16x8*)phi, hi1 = *(LAS bf16x8*)(phi + 64);
    const int off = WS_P2 + g * 32768 + (j * 8 + wv * 2) * 1024 + (lane & 7) * 128 + (lane & 8) * 8 + G * 16;
#pragma unroll
    for (int a = 0; a < 2; ++a) {
      f32x4 c = {};
      c = __builtin_amdgcn_mfma_f32_16x16x32_bf16(wf[2 * a][0], lo0, c, 0, 0, 0);
      c = __builtin_amdgcn_mfma_f32_16x16x32_bf16(wf[2 * a][1], lo1, c, 0, 0, 0);
      c = __builtin_amdgcn_mfma_f32_16x16x32_bf16(wf[2 * a + 1][0], hi0, c, 0, 0, 0);
      c = __builtin_amdgcn_mfma_f32_16x16x32_bf16(wf[2 * a + 1][1], hi1, c, 0, 0, 0);
      __builtin_amdgcn_raw_buffer_store_b128(tag_chunk(c[0], c[1], c[2], c[3], tb), rw, off + a * 1024, 0, AUX_SC1);
    }
  }
  STAMP(8);

  if (wv == 2 || (wv == 3 && g < 8)) {
    const int t = g + (wv - 2) * NGROUP, m = t & 7, cb = t >> 3;
    const unsigned sv = *(LAS unsigned*)(smem + L_SEL + m * 4);
    const int tok = m * 4 + (sv & 3u);
    const int c8 = lane & 7;
    const v2u xr = *(LAS v2u*)(smem + L_X + tok * 512 + (((cb * 4 + (c8 >> 1)) ^ (tok & 15)) * 16) + (c8 & 1) * 8);
    const float rs = bf_lo(pack_bf16(sigmoidf(bf_hi(sv)), 0.f));
    f32x4 y = gather_sum<7, 8 * 32768>(rw, WS_P2 + (lane >> 3) * 32768 + (j * 8 + cb) * 1024 + m * 128 + c8 * 16, tb, ok);
    STAMP(9);
#pragma unroll
    for (int e = 0; e < 4; ++e) { y[e] += __shfl_xor(y[e], 8); y[e] += __shfl_xor(y[e], 16); y[e] += __shfl_xor(y[e], 32); }
    v2u o;
#pragma unroll
    for (int e = 0; e < 2; ++e) {
      const unsigned yp = pack_bf16(y[2 * e], y[2 * e + 1]);
      const unsigned pp = pack_bf16(bf_lo(yp) * rs, bf_hi(yp) * rs);
      const float x0 = bf_lo(xr[e]), x1 = bf_hi(xr[e]);
      o[e] = pack_bf16(x0 + (x0 * 0.0f + bf_lo(pp)), x1 + (x1 * 0.0f + bf_hi(pp)));
    }
    if (lane < 8) *(GAS v2u*)(out + tok * D + j * 256 + cb * 32 + c8 * 4) = o;
    if (wk == 0 && wv == 2 && lane == 0 && ok) __hip_atomic_store((GAS unsigned*)ws, tag, RLX_AGENT);
    STAMP(10);
  }
  if (wv == 2) { asm volatile("s_waitcnt lgkmcnt(0)" : "+s"(tstart)); while (__builtin_amdgcn_s_memrealtime() - tstart < 2000ull) __builtin_amdgcn_s_sleep(1); }
  STAMP_OUT();
}
}

extern "C" void kernel_launch(void* const* d_in, const int* in_sizes, int n_in, void* d_out, int out_size, void* d_ws, size_t ws_size,
                              hipStream_t stream) {
  mod_ffn_kernel<<<GRID, 1024, 0, stream>>>((const u16*)d_in[0], (const u16*)d_in[1], (const u16*)d_in[2], (const u16*)d_in[3],
                                           (const u16*)d_in[4], (u16*)d_out, (char*)d_ws);
}
```

```cpp
#include <hip/hip_runtime.h>
#include <stdint.h>

#define WAUX " nt"
#define THR 12
#define GREM 6
#define SELDELAY 4
#define H2DELAY 12

namespace {
typedef unsigned short u16;
typedef unsigned long long u64;
typedef unsigned v4u __attribute__((ext_vector_type(4)));
typedef unsigned v2u __attribute__((ext_vector_type(2)));
typedef float f32x2 __attribute__((ext_vector_type(2)));
typedef float f32x4 __attribute__((ext_vector_type(4)));
typedef float f32x16 __attribute__((ext_vector_type(16)));
typedef short s16x4 __attribute__((ext_vector_type(4)));
typedef __bf16 bf16x2 __attribute__((ext_vector_type(2)));
typedef __bf16 bf16x8 __attribute__((ext_vector_type(8)));
#define GAS __attribute__((address_space(1)))
#define LAS __attribute__((address_space(3)))
#define RLX_AGENT __ATOMIC_RELAXED, __HIP_MEMORY_SCOPE_AGENT

constexpr int D = 1024, F = 3584;
constexpr int NROUTER = 8, NGROUP = 56, GRID = NROUTER + 4 * NGROUP;
constexpr int W_BYTES = D * F * 2, X_BYTES = 32 * D * 2;
constexpr int WS_SEL = 1024, WS_P1 = 4096, WS_P2 = WS_P1 + 4 * NGROUP * 4096, WS_BYTES = WS_P2 + NGROUP * 32768;
constexpr int L_W1 = 0, L_WD = 65536, L_X = 98304, L_H = 114688, H_ROW = 144, L_Z = L_H + 8 * H_ROW, L_SEL = L_Z + 128,
              L_EX = L_SEL + 64, L_BYTES = L_EX + 256;
constexpr int AUX_SC1 = 16;
constexpr unsigned SPIN_CAP = 1u << 16;
constexpr int NITEM = 48;
constexpr int CODE_LINES = 56;

#define STAMP(i)
#define STAMP_VAL(i, v)
#define STAMP_OUT()

__device__ __forceinline__ v4u srd(const void* p, unsigned bytes) {
  const u64 a = (u64)p;
  return v4u{(unsigned)a, (unsigned)(a >> 32) & 0xffffu, bytes, 0x00020000u};
}
template <int LOFF, int GOFF> __device__ __forceinline__ void dma_w(unsigned lds, unsigned voff, v4u rs) {
  unsigned t;
  asm volatile("s_add_u32 m0, %1, %4\n\ts_mov_b32 %0, %5\n\tbuffer_load_dwordx4 %2, %3, %0 offen" WAUX " lds"
               : "=&s"(t) : "s"(lds), "v"(voff), "s"(rs), "i"(LOFF), "i"(GOFF) : "scc", "memory");
}
template <int LOFF, int GOFF> __device__ __forceinline__ void dma_x(unsigned lds, unsigned voff, v4u rs) {
  unsigned t;
  asm volatile("s_add_u32 m0, %1, %4\n\ts_mov_b32 %0, %5\n\tbuffer_load_dwordx4 %2, %3, %0 offen lds"
               : "=&s"(t) : "s"(lds), "v"(voff), "s"(rs), "i"(LOFF), "i"(GOFF) : "scc", "memory");
}
template <int N> __device__ __forceinline__ void wait_vm() { asm volatile("s_waitcnt vmcnt(%0)" ::"i"(N) : "memory"); }
__device__ __forceinline__ void wg_barrier() { asm volatile("s_waitcnt lgkmcnt(0)\n\ts_barrier" ::: "memory"); }

__device__ __forceinline__ bf16x8 tr_frag(LAS char* p) {
  const s16x4 lo = __builtin_amdgcn_ds_read_tr16_b64_v4i16((LAS s16x4*)p);
  const s16x4 hi = __builtin_amdgcn_ds_read_tr16_b64_v4i16((LAS s16x4*)(p + 512));
  return __builtin_bit_cast(bf16x8, __builtin_shufflevector(lo, hi, 0, 1, 2, 3, 4, 5, 6, 7));
}
__device__ __forceinline__ unsigned pack_bf16(float a, float b) {
  return __builtin_bit_cast(unsigned, __builtin_convertvector(f32x2{a, b}, bf16x2));
}
__device__ __forceinline__ unsigned as_u(float v) { return __builtin_bit_cast(unsigned, v); }
__device__ __forceinline__ float as_f(unsigned v) { return __builtin_bit_cast(float, v); }
__device__ __forceinline__ float bf_lo(unsigned p) { return as_f(p << 16); }
__device__ __forceinline__ float bf_hi(unsigned p) { return as_f(p & 0xffff0000u); }
__device__ __forceinline__ float sigmoidf(float v) { return __builtin_amdgcn_rcpf(1.0f + __expf(-v)); }
__device__ __forceinline__ float dot2(unsigned a, unsigned b, float c) {
  return __builtin_amdgcn_fdot2_f32_bf16(__builtin_bit_cast(bf16x2, a), __builtin_bit_cast(bf16x2, b), c, false);
}
template <int CTRL> __device__ __forceinline__ float dpp_add(float v) {
  return v + as_f(__builtin_amdgcn_update_dpp(0u, as_u(v), CTRL, 0xf, 0xf, true));
}
__device__ __forceinline__ float swap16_add(float v) {
  const v2u r = __builtin_amdgcn_permlane16_swap(as_u(v), as_u(v), false, false);
  return as_f(r.x) + as_f(r.y);
}
__device__ __forceinline__ float swap32_add(float v) {
  const v2u r = __builtin_amdgcn_permlane32_swap(as_u(v), as_u(v), false, false);
  return as_f(r.x) + as_f(r.y);
}
constexpr int DPP_XOR1 = 0xb1, DPP_XOR2 = 0x4e, DPP_HALF_MIRROR = 0x141, DPP_ROR8 = 0x128;

__device__ __forceinline__ v4u tag_chunk(float a, float b, float c, float d, unsigned tagv) {
  return v4u{__builtin_amdgcn_perm(as_u(a), tagv, 0x07060500u), __builtin_amdgcn_perm(as_u(b), tagv, 0x07060501u),
             __builtin_amdgcn_perm(as_u(c), tagv, 0x07060502u), __builtin_amdgcn_perm(as_u(d), tagv, 0x07060503u)};
}
template <int N, int STRIDE> __device__ __forceinline__ void gather_issue(v4u (&c)[N], __amdgpu_buffer_rsrc_t rs, int off) {
#pragma unroll
  for (int i = 0; i < N; ++i) c[i] = __builtin_amdgcn_raw_buffer_load_b128(rs, off, i * STRIDE, AUX_SC1);
}
template <int N, int STRIDE>
__device__ __forceinline__ f32x4 gather_finish(v4u (&c)[N], __amdgpu_buffer_rsrc_t rs, int off, v4u tb, bool& ok, unsigned& rounds) {
  f32x4 sum;
#pragma nounroll
  for (unsigned spins = 0;; ++spins) {
    unsigned bad = 0;
#pragma unroll
    for (int i = 0; i < N; ++i) {
      const v4u v = c[i] ^ tb;
      bad |= v.x | v.y | v.z | v.w;
      sum = i ? sum + __builtin_bit_cast(f32x4, v) : __builtin_bit_cast(f32x4, v);
    }
    rounds = spins + 1;
    if (__builtin_amdgcn_ballot_w64((bad & 0xffu) != 0) == 0) break;
    if (spins > SPIN_CAP) { ok = false; break; }
    asm volatile("" ::: "memory");
    gather_issue<N, STRIDE>(c, rs, off);
  }
  return sum;
}

struct Stream { unsigned l1, v1, ld, vd0, vd1; v4u r1, rd; };
template <int I> __device__ __forceinline__ void stream_item(const Stream& s) {
  if constexpr (I < 32) dma_w<I * 1024, I * 8 * F * 2>(s.l1, s.v1, s.r1);
  else if constexpr (I < NITEM) {
    constexpr int k = I - 32, piece = k >> 1, sl = k & 1;
    dma_w<sl * 8192 + piece * 1024, piece * 8 * D * 2 + sl * 128>(s.ld, (piece & 1) ? s.vd1 : s.vd0, s.rd);
  }
}
template <int LO, int HI> __device__ __forceinline__ void stream_range(const Stream& s) {
  if constexpr (LO < HI) { stream_item<LO>(s); stream_range<LO + 1, HI>(s); }
}
template <int I> __device__ __forceinline__ void x_range(unsigned lx, unsigned vx, v4u rx) {
  if constexpr (I < 8) { dma_x<I * 1024, I * 2 * D * 2>(lx, vx ^ (I * 32), rx); x_range<I + 1>(lx, vx, rx); }
}
constexpr int LEAD = THR / 2;
template <int SS> __device__ __forceinline__ void lead_steps(const Stream& s) {
  if constexpr (SS < LEAD) {
    wait_vm<THR + 8 - 2>();
    stream_range<THR + 2 * SS, THR + 2 * SS + 2>(s);
    lead_steps<SS + 1>(s);
  }
}
template <int K> __device__ __forceinline__ void mma_step(LAS char* a0, LAS char* a1, const bf16x8 (&xf)[16], f32x16& c0, f32x16& c1) {
  const bf16x8 f0 = tr_frag(a0 + K * 2048), f1 = tr_frag(a1 + K * 2048);
  c0 = __builtin_amdgcn_mfma_f32_32x32x16_bf16(f0, xf[K], c0, 0, 0, 0);
  c1 = __builtin_amdgcn_mfma_f32_32x32x16_bf16(f1, xf[K], c1, 0, 0, 0);
}
template <int SS> __device__ __forceinline__ void phase1_steps(const Stream& s, LAS char* a0, LAS char* a1, const bf16x8 (&xf)[16], f32x16& c0, f32x16& c1) {
  if constexpr (SS < 16) {
    constexpr int issued = THR + 2 * SS < NITEM ? THR + 2 * SS : NITEM;
    if constexpr (SS - LEAD < LEAD) { mma_step<SS - LEAD>(a0, a1, xf, c0, c1); __builtin_amdgcn_sched_barrier(0); }
    wait_vm<issued - (2 * SS + 2)>();
    if constexpr (SS < 15) stream_range<THR + 2 * SS, THR + 2 * SS + 2>(s);
    mma_step<SS>(a0, a1, xf, c0, c1);
    __builtin_amdgcn_sched_barrier(0);
    if constexpr (SS == 14) wg_barrier();
    phase1_steps<SS + 1>(s, a0, a1, xf, c0, c1);
  }
}
constexpr int ISSUED1 = THR + 30 < NITEM ? THR + 30 : NITEM;
static_assert(THR % 2 == 0 && THR >= 4 && THR <= 16 && GREM <= 16, "stream window");

__global__ void __launch_bounds__(1024)
mod_ffn_kernel(const u16* __restrict__ x, const u16* __restrict__ wr, const u16* __restrict__ wg, const u16* __restrict__ wu,
               const u16* __restrict__ wd, u16* __restrict__ out, char* ws) {
  __shared__ __attribute__((aligned(1024))) char smem_[L_BYTES];
  LAS char* smem = (LAS char*)smem_;
  const int lane = threadIdx.x & 63, wv = __builtin_amdgcn_readfirstlane(threadIdx.x >> 6);
  u64 pc0;
  asm volatile("s_getpc_b64 %0" : "=s"(pc0));
  const int wk = blockIdx.x - NROUTER, j = wk / NGROUP, g = wk - j * NGROUP;
  const unsigned lds0 = (unsigned)(size_t)smem;
  const unsigned sw = (lane & 7) ^ ((lane >> 2) & 4);
  const unsigned v1 = (j * 256 + (lane >> 3)) * (F * 2) + g * 128 + sw * 16;
  asm volatile("" ::"v"(v1), "s"(wv), "s"(x), "s"(wr), "s"(wg), "s"(wu), "s"(wd), "s"(out), "s"(ws));
  unsigned epoch, tag;
  asm volatile("s_load_dword %0, %1, 0x0" : "=s"(epoch) : "s"(ws) : "memory");
#define TAG_READY() asm volatile("s_waitcnt lgkmcnt(0)\n\ts_add_u32 %0, %1, 0x01010101" : "=s"(tag) : "s"(epoch) : "scc", "memory")
  bool ok = true;
  unsigned rounds = 0;
  const __amdgpu_buffer_rsrc_t rw = __builtin_amdgcn_make_buffer_rsrc((void*)ws, 0, WS_BYTES, 0x00020000);
  const int q = (lane >> 2) & 3, p = lane & 3, G = lane >> 4;

  if (__builtin_expect(wk >= 0 && wv < 2, 1)) {
    Stream s;
    s.r1 = srd(wv ? wu : wg, W_BYTES);
    s.v1 = v1;
    s.l1 = lds0 + L_W1 + wv * 32768;
    stream_range<0, THR>(s);
    __builtin_amdgcn_sched_barrier(0);
    x_range<0>(lds0 + L_X + wv * 8192, (wv * 16 + (lane >> 5)) * (D * 2) + j * 512 + (((lane & 31) ^ (lane >> 5)) * 16), srd(x, X_BYTES));
    __builtin_amdgcn_sched_barrier(0);
    STAMP(1);
    s.rd = srd(wd, W_BYTES);
    s.vd0 = (g * 64 + (lane >> 3)) * (D * 2) + j * 512 + wv * 256 + sw * 16;
    s.vd1 = s.vd0 ^ 32;
    s.ld = lds0 + L_WD + wv * 16384;
    TAG_READY();
    const unsigned xa = (lane & 31) * 512 + (((lane >> 5) ^ (lane & 15)) * 16);
    const unsigned ta = (8 * (G >> 1) + q) * 128 + ((32 * (G & 1) + 8 * p) ^ (64 * (q >> 1)));
    const int b = (lane & 31) >> 2;
    const int off = WS_P1 + (g * 4 + j) * 4096 + wv * 2048 + (b * 2 + (lane >> 5)) * 16;
    unsigned tagv = tag;
    asm volatile("" : "+v"(tagv));
    lead_steps<0>(s);
    wait_vm<THR>();
    wg_barrier();
    STAMP(2);
    bf16x8 xf[16];
#pragma unroll
    for (int k = 0; k < 16; ++k) xf[k] = *(LAS bf16x8*)(smem + L_X + (xa ^ (k * 32)));
    f32x16 c0 = {}, c1 = {};
    phase1_steps<LEAD>(s, smem + L_W1 + wv * 32768 + ta, smem + L_W1 + wv * 32768 + (ta ^ 64), xf, c0, c1);
    STAMP(3);
    if ((*(LAS unsigned*)(smem + L_SEL + b * 4) & 3u) == (unsigned)(lane & 3)) {
#pragma unroll
      for (int k = 0; k < 4; ++k)
        __builtin_amdgcn_raw_buffer_store_b128(tag_chunk(c0[4 * k], c0[4 * k + 1], c0[4 * k + 2], c0[4 * k + 3], tagv), rw, off + k * 256, 0, AUX_SC1);
#pragma unroll
      for (int k = 0; k < 4; ++k)
        __builtin_amdgcn_raw_buffer_store_b128(tag_chunk(c1[4 * k], c1[4 * k + 1], c1[4 * k + 2], c1[4 * k + 3], tagv), rw, off + 1024 + k * 256, 0, AUX_SC1);
    }
    STAMP(4);
    stream_range<ISSUED1, NITEM>(s);
    wait_vm<(GREM > NITEM - ISSUED1 ? GREM + 8 : GREM)>();
  } else if (wk >= 0) {
    if (lane < 32) *(LAS unsigned*)(smem + L_Z + lane * 4) = 0u;
    TAG_READY();
    STAMP(1);
    wg_barrier();
    if (wv == 2) {
      __builtin_amdgcn_s_sleep(SELDELAY);
#pragma nounroll
      for (unsigned spins = 0;; ++spins) {
        const u64 v = __hip_atomic_load((GAS u64*)(ws + WS_SEL) + (lane & 7), RLX_AGENT);
        rounds = spins + 1;
        if (__builtin_amdgcn_ballot_w64((unsigned)(v >> 32) != tag) == 0) { if (lane < 8) *(LAS unsigned*)(smem + L_SEL + lane * 4) = (unsigned)v; break; }
        if (spins > SPIN_CAP) { ok = false; break; }
      }
      STAMP_VAL(13, rounds);
    }
    STAMP(2);
    wg_barrier();
    STAMP(3);
  } else {
    if (wv == 1) {
      const unsigned t0 = *(const GAS unsigned*)((pc0 & ~(u64)255) + (lane < CODE_LINES ? lane : 0) * 128);
      asm volatile("" ::"v"(t0));
    }
    if (wv != 0) return;
    const int b = blockIdx.x;
    const GAS v4u* xp = (const GAS v4u*)(x + b * 4 * D) + lane;
    const GAS v4u* wp = (const GAS v4u*)wr + lane;
    v4u xv[4][2], wv2[2];
    wv2[0] = wp[0]; wv2[1] = wp[64];
#pragma unroll
    for (int s = 0; s < 4; ++s) { xv[s][0] = xp[s * 128]; xv[s][1] = xp[s * 128 + 64]; }
    TAG_READY();
    int sel = 0; float best = 0.f;
#pragma unroll
    for (int s = 0; s < 4; ++s) {
      float a = 0.f;
#pragma unroll
      for (int i = 0; i < 2; ++i)
#pragma unroll
        for (int e = 0; e < 4; ++e) a = dot2(xv[s][i][e], wv2[i][e], a);
      a = swap32_add(swap16_add(dpp_add<DPP_ROR8>(dpp_add<DPP_HALF_MIRROR>(dpp_add<DPP_XOR2>(dpp_add<DPP_XOR1>(a))))));
      const float r = bf_lo(pack_bf16(a, 0.f));
      const bool take = s == 0 || r > best || (best != best && r == r);
      sel = take ? s : sel; best = take ? r : best;
    }
    if (lane == 0) __hip_atomic_store((GAS u64*)(ws + WS_SEL) + b, ((u64)tag << 32) | as_u(best) | (unsigned)sel, RLX_AGENT);
    STAMP(1);
    sel = __builtin_amdgcn_readfirstlane(sel);
    GAS v4u* op = (GAS v4u*)(out + b * 4 * D) + lane;
#pragma unroll
    for (int s = 0; s < 4; ++s)
      if (s != sel) {
#pragma unroll
        for (int i = 0; i < 2; ++i) {
          v4u o;
#pragma unroll
          for (int e = 0; e < 4; ++e) { const float lo = bf_lo(xv[s][i][e]), hi = bf_hi(xv[s][i][e]); o[e] = pack_bf16(lo + lo * 0.0f, hi + hi * 0.0f); }
          op[s * 128 + i * 64] = o;
        }
      }
    STAMP_OUT();
    return;
  }
  const v4u tb = v4u{tag & 255u, (tag >> 8) & 255u, (tag >> 16) & 255u, tag >> 24};
  unsigned tagv = tag;
  asm volatile("" : "+v"(tagv));
  const int pos = wv * 32 + (lane & 31);
  const int off1 = WS_P1 + g * 16384 + (lane >> 5) * 2048 + pos * 16;
  wg_barrier();
  STAMP(5);

  v4u c1[4];
  gather_issue<4, 4096>(c1, rw, off1);
  const unsigned da = (8 * G + q) * 128 + ((8 * p) ^ (64 * (q >> 1)) ^ (32 * (G & 1)));
  const unsigned ha = L_H + (lane & 7) * H_ROW + G * 16;
  LAS char* plo = smem + ((lane & 8) ? L_Z : ha);
  LAS char* phi = smem + ((lane & 8) ? ha : L_Z);
  LAS char* hw = smem + L_H + ((pos >> 1) & 7) * H_ROW + ((pos >> 4) * 8 + (pos & 1) * 4 + (lane >> 5) * 2) * 2;
  const int off2 = WS_P2 + g * 32768 + (j * 8 + wv * 2) * 1024 + (lane & 7) * 128 + (lane & 8) * 8 + G * 16;
  wait_vm<4>();
  wg_barrier();
  STAMP(11);
  bf16x8 wf[4][2];
#pragma unroll
  for (int ct = 0; ct < 4; ++ct)
#pragma unroll
    for (int ks = 0; ks < 2; ++ks) wf[ct][ks] = tr_frag(smem + L_WD + wv * 8192 + (da ^ (32 * ct)) + ks * 4096);
  const f32x4 s1 = gather_finish<4, 4096>(c1, rw, off1, tb, ok, rounds);
  STAMP(6);
  STAMP_VAL(14, rounds);
  {
    const v2u e = __builtin_amdgcn_permlane32_swap(as_u(s1[0]), as_u(s1[2]), false, false);
    const v2u o = __builtin_amdgcn_permlane32_swap(as_u(s1[1]), as_u(s1[3]), false, false);
    const unsigned gp = pack_bf16(as_f(e.x), as_f(o.x));
    const unsigned up = pack_bf16(as_f(e.y), as_f(o.y));
    const float g0 = bf_lo(gp), g1 = bf_hi(gp);
    const unsigned sp = pack_bf16(g0 * sigmoidf(g0), g1 * sigmoidf(g1));
    *(LAS unsigned*)hw = pack_bf16(bf_lo(sp) * bf_lo(up), bf_hi(sp) * bf_hi(up));
  }
  wg_barrier();
  STAMP(7);

  {
    const bf16x8 lo0 = *(LAS bf16x8*)plo, lo1 = *(LAS bf16x8*)(plo + 64), hi0 = *(LAS bf16x8*)phi, hi1 = *(LAS bf16x8*)(phi + 64);
#pragma unroll
    for (int a = 0; a < 2; ++a) {
      f32x4 c = {};
      c = __builtin_amdgcn_mfma_f32_16x16x32_bf16(wf[2 * a][0], lo0, c, 0, 0, 0);
      c = __builtin_amdgcn_mfma_f32_16x16x32_bf16(wf[2 * a][1], lo1, c, 0, 0, 0);
      c = __builtin_amdgcn_mfma_f32_16x16x32_bf16(wf[2 * a + 1][0], hi0, c, 0, 0, 0);
      c = __builtin_amdgcn_mfma_f32_16x16x32_bf16(wf[2 * a + 1][1], hi1, c, 0, 0, 0);
      __builtin_amdgcn_raw_buffer_store_b128(tag_chunk(c[0], c[1], c[2], c[3], tagv), rw, off2 + a * 1024, 0, AUX_SC1);
    }
  }
  STAMP(8);

  if (wv < 2 || g < 8) {
    const int t = g + (wv >> 1) * NGROUP, m = t & 7, cb = t >> 3;
    const int c8 = lane & 7;
    const int off3 = WS_P2 + ((wv & 1) * 28 + (lane >> 3)) * 32768 + (j * 8 + cb) * 1024 + m * 128 + c8 * 16;
    const int off4 = off3 + (lane < 32 ? 24 : 16) * 32768;
    const unsigned sv = *(LAS unsigned*)(smem + L_SEL + m * 4);
    const int tok = m * 4 + (sv & 3u);
    const v2u xr = *(LAS v2u*)(smem + L_X + tok * 512 + (((cb * 4 + (c8 >> 1)) ^ (tok & 15)) * 16) + (c8 & 1) * 8);
    const float rs = bf_lo(pack_bf16(sigmoidf(bf_hi(sv)), 0.f));
    asm volatile("" ::"v"(xr), "v"(rs));
    __builtin_amdgcn_s_sleep(H2DELAY);
    f32x4 y;
#pragma nounroll
    for (unsigned spins = 0;; ++spins) {
      v4u c2[4];
#pragma unroll
      for (int i = 0; i < 3; ++i) c2[i] = __builtin_amdgcn_raw_buffer_load_b128(rw, off3, i * 8 * 32768, AUX_SC1);
      c2[3] = __builtin_amdgcn_raw_buffer_load_b128(rw, off4, 0, AUX_SC1);
      unsigned bad = 0;
#pragma unroll
      for (int i = 0; i < 4; ++i) { c2[i] ^= tb; bad |= c2[i].x | c2[i].y | c2[i].z | c2[i].w; }
      if (lane >= 32) c2[3] = v4u{0u, 0u, 0u, 0u};
      y = (__builtin_bit_cast(f32x4, c2[0]) + __builtin_bit_cast(f32x4, c2[1])) + (__builtin_bit_cast(f32x4, c2[2]) + __builtin_bit_cast(f32x4, c2[3]));
      rounds = spins + 1;
      if (__builtin_amdgcn_ballot_w64((bad & 0xffu) != 0) == 0) break;
      if (spins > SPIN_CAP) { ok = false; break; }
      asm volatile("" ::: "memory");
    }
    STAMP(9);
    STAMP_VAL(15, rounds);
#pragma unroll
    for (int e = 0; e < 4; ++e) y[e] = swap32_add(swap16_add(dpp_add<DPP_ROR8>(y[e])));
    LAS f32x4* ex = (LAS f32x4*)(smem + L_EX + (wv >> 1) * 128 + c8 * 16);
    if (wv & 1) { if (lane < 8) *ex = y; if (!ok) *(LAS unsigned*)(smem + L_Z + 124) = 1u; }
    wg_barrier();
    if (!(wv & 1)) {
      y += *ex;
      v2u o;
#pragma unroll
      for (int e = 0; e < 2; ++e) {
        const unsigned yp = pack_bf16(y[2 * e], y[2 * e + 1]);
        const unsigned pp = pack_bf16(bf_lo(yp) * rs, bf_hi(yp) * rs);
        const float x0 = bf_lo(xr[e]), x1 = bf_hi(xr[e]);
        o[e] = pack_bf16(x0 + (x0 * 0.0f + bf_lo(pp)), x1 + (x1 * 0.0f + bf_hi(pp)));
      }
      if (lane < 8) *(GAS v2u*)(out + tok * D + j * 256 + cb * 32 + c8 * 4) = o;
      if (wk == 0 && wv == 0 && lane == 0 && ok && *(LAS unsigned*)(smem + L_Z + 124) == 0u) __hip_atomic_store((GAS unsigned*)ws, tag, RLX_AGENT);
    }
    STAMP(10);
  }
  STAMP_OUT();
}
}

extern "C" void kernel_launch(void* const* d_in, const int* in_sizes, int n_in, void* d_out, int out_size, void* d_ws, size_t ws_size,
                              hipStream_t stream) {
  mod_ffn_kernel<<<GRID, 256, 0, stream>>>((const u16*)d_in[0], (const u16*)d_in[1], (const u16*)d_in[2], (const u16*)d_in[3],
                                           (const u16*)d_in[4], (u16*)d_out, (char*)d_ws);
}
```

```cpp
#include <hip/hip_runtime.h>

namespace {
constexpr unsigned D = 1024, F = 3584, TOKENS = 32;
constexpr unsigned GRID = 16 + 4 * 56, LDS_BYTES = 115968, WS_BYTES = 6029312;
constexpr unsigned SRD3 = 0x00020000u;

__global__ void __attribute__((aligned(4096))) __launch_bounds__(256, 1)
mod_ffn_kernel(const void* wg, unsigned n0, unsigned f0, const void* wu, unsigned n1, unsigned f1, const void* x, unsigned n2, unsigned f2,
               const void* wd, unsigned n3, unsigned f3, void* ws, unsigned n4, unsigned f4, void* out, unsigned n5, unsigned f5,
               const void* wr, unsigned n6, unsigned f6) {
  __shared__ __attribute__((aligned(4096))) char smem[LDS_BYTES];
  asm volatile(
      "; ==== entry. s4 = 64 * wave, v3 = lane; descriptors: s[12:15] gate or up (w_r in the leading blocks),\n"
      ";      s[16:19] x, s[20:23] w_down, s[24:27] workspace, s[28:31] out\n"
      "s_load_dwordx16 s[16:31], %0, 0x20\n"
      "s_and_b32 s84, %1, 1   ; matrix of phase 1\n"
      "s_lshl_b32 s10, s84, 4\n"
      "s_sub_u32 s6, %1, 16\n"
      "s_cselect_b32 s10, 0x60, s10\n"
      "s_load_dwordx4 s[12:15], %0, s10\n"
      "v_readfirstlane_b32 s4, %2\n"
      "v_and_b32 v3, 63, %2\n"
      "s_cbranch_scc1 .Llead\n"
      "; pull the rest of this page of code into L2 (range-checked: lanes past the end read nothing)\n"
      "s_getpc_b64 s[80:81]\n"
      ".Lpc:\n"
      "s_mov_b32 s82, .Lhot_end-.Lpc\n"
      "s_mov_b32 s83, 0x20000\n"
      "v_lshlrev_b32 v1, 7, %2\n"
      "buffer_load_dword v2, v1, s[80:83], 0 offen\n"
      "; ==== worker (g = s6, j = s7). s5 = wave & 1, s43 = wave >> 1, s85 = group that this wave computes partial sums for\n"
      "s_mul_i32 s7, s6, 1171\n"
      "s_lshr_b32 s7, s7, 16\n"
      "s_mul_i32 s10, s7, 56\n"
      "s_sub_u32 s6, s6, s10\n"
      "s_bfe_u32 s5, s4, 0x10006\n"
      "s_lshr_b32 s43, s4, 7\n"
      "s_xor_b32 s85, s6, s84\n"
      "s_or_b32 s85, s85, s5\n"
      "s_lshl_b32 s10, s7, 5\n"
      "s_or_b32 s10, s10, s43\n"
      "s_mul_i32 s8, s10, 0xe000\n"
      "s_lshl_b32 s10, s85, 7\n"
      "s_add_u32 s8, s8, s10   ; first piece in memory\n"
      "s_lshl_b32 s44, s5, 15\n"
      "s_add_u32 s44, s44, %3   ; tile [256 rows][128 B] in LDS\n"
      "s_lshl_b32 s37, s43, 10\n"
      "s_add_u32 s37, s37, s44\n"
      "s_mov_b32 m0, s37\n"
      "s_mov_b32 s9, 0x1c000\n"
      "v_lshrrev_b32 v4, 3, v3\n"
      "v_and_b32 v62, 7, v3\n"
      "v_lshrrev_b32 v58, 4, v3\n"
      "v_lshrrev_b32 v15, 2, v3\n"
      "v_and_b32 v5, 4, v15\n"
      "v_xor_b32 v5, v5, v62\n"
      "v_lshlrev_b32 v5, 4, v5   ; 16-byte slot, halves swapped on rows with bit 1 set\n"
      "v_mul_u32_u24 v1, 0x1c00, v4\n"
      "v_add_u32 v1, v1, v5\n"
      "; activations: four chunks [32 tokens][64 channels]; wave w moves tokens 8w..8w+7, slot XORed with token >> 1\n"
      "s_lshl_b32 s10, s5, 2\n"
      "v_or_b32 v6, s10, v58\n"
      "v_xor_b32 v6, v6, v62\n"
      "v_lshlrev_b32 v6, 4, v6\n"
      "v_lshl_or_b32 v6, v4, 11, v6\n"
      "s_lshl_b32 s45, s7, 9\n"
      "s_lshl_b32 s46, s4, 8\n"
      "s_add_u32 s46, s46, s45\n"
      "s_add_u32 s47, %3, 0x18000\n"
      "s_lshl4_add_u32 s36, s4, s47\n"
      "s_waitcnt lgkmcnt(0)\n"
      "; weight stream: the wave moves every other 8-row piece of its tile and keeps up to 7 of them in flight\n"
      "buffer_load_dwordx4 v1, s[12:15], s8 offen lds\n"
      "s_load_dword s11, s[24:25], 0x0   ; epoch word\n"
      "s_mov_b32 m0, s36\n"
      "s_addk_i32 s36, 0xf80\n"
      "buffer_load_dwordx4 v6, s[16:19], s46 offen offset:0 lds\n"
      "s_add_u32 m0, s37, 0x800\n"
      "s_add_u32 s8, s8, s9\n"
      "buffer_load_dwordx4 v1, s[12:15], s8 offen lds\n"
      "s_addk_i32 m0, 0x800\n"
      "s_add_u32 s8, s8, s9\n"
      "buffer_load_dwordx4 v1, s[12:15], s8 offen lds\n"
      "s_addk_i32 m0, 0x800\n"
      "s_add_u32 s8, s8, s9\n"
      "buffer_load_dwordx4 v1, s[12:15], s8 offen lds\n"
      "s_mov_b32 m0, s36\n"
      "s_addk_i32 s36, 0xf80\n"
      "buffer_load_dwordx4 v6, s[16:19], s46 offen offset:128 lds\n"
      "s_add_u32 m0, s37, 0x2000\n"
      "s_add_u32 s8, s8, s9\n"
      "buffer_load_dwordx4 v1, s[12:15], s8 offen lds\n"
      "s_branch .Lworker2\n"
      "; ==== leading blocks, index s6 = 0..7 within their kind: blocks 0-3, 8-11 route, blocks 4-7, 12-15 vote\n"
      ".Llead:\n"
      "s_lshr_b32 s7, %1, 1\n"
      "s_and_b32 s7, s7, 4\n"
      "s_and_b32 s6, %1, 3\n"
      "s_or_b32 s6, s6, s7\n"
      "s_lshl_b32 s9, s6, 4\n"
      "v_lshlrev_b32 v1, 4, v3\n"
      "s_lshl_b32 s8, s6, 8\n"
      "s_add_u32 s8, s8, s4\n"
      "s_lshl_b32 s8, s8, 5\n"
      "s_bitcmp1_b32 %1, 2\n"
      "s_waitcnt lgkmcnt(0)\n"
      "s_load_dword s11, s[24:25], 0x0\n"
      "s_memrealtime s[70:71]\n"
      "s_cbranch_scc1 .Lvote\n"
      "; router of sequence s6: wave w scores token w\n"
      "buffer_load_dwordx4 v[4:7], v1, s[16:19], s8 offen\n"
      "buffer_load_dwordx4 v[12:15], v1, s[12:15], 0 offen\n"
      "buffer_load_dwordx4 v[8:11], v1, s[16:19], s8 offen offset:1024\n"
      "buffer_load_dwordx4 v[16:19], v1, s[12:15], 0 offen offset:1024\n"
      "s_lshr_b32 s10, s4, 4\n"
      "v_mov_b32 v23, %3\n"
      "v_add_u32 v22, s10, v23\n"
      "v_and_b32 v24, 3, v3\n"
      "v_lshl_add_u32 v24, v24, 2, v23\n"
      "v_mov_b32 v20, 0\n"
      "s_waitcnt vmcnt(0)\n"
      "v_dot2c_f32_bf16 v20, v4, v12\n"
      "v_dot2c_f32_bf16 v20, v5, v13\n"
      "v_dot2c_f32_bf16 v20, v6, v14\n"
      "v_dot2c_f32_bf16 v20, v7, v15\n"
      "v_dot2c_f32_bf16 v20, v8, v16\n"
      "v_dot2c_f32_bf16 v20, v9, v17\n"
      "v_dot2c_f32_bf16 v20, v10, v18\n"
      "v_dot2c_f32_bf16 v20, v11, v19\n"
      "s_nop 2\n"
      "v_add_f32_dpp v20, v20, v20 quad_perm:[1,0,3,2] row_mask:0xf bank_mask:0xf\n"
      "s_nop 1\n"
      "v_add_f32_dpp v20, v20, v20 quad_perm:[2,3,0,1] row_mask:0xf bank_mask:0xf\n"
      "s_nop 1\n"
      "v_add_f32_dpp v20, v20, v20 row_half_mirror row_mask:0xf bank_mask:0xf\n"
      "s_nop 1\n"
      "v_add_f32_dpp v20, v20, v20 row_ror:8 row_mask:0xf bank_mask:0xf\n"
      "v_mov_b32 v21, v20\n"
      "s_nop 1\n"
      "v_permlane16_swap_b32 v20, v21\n"
      "v_add_f32 v20, v20, v21\n"
      "v_mov_b32 v21, v20\n"
      "s_nop 1\n"
      "v_permlane32_swap_b32 v20, v21\n"
      "v_add_f32 v20, v20, v21\n"
      "v_cvt_pk_bf16_f32 v20, 0, v20\n"
      "ds_write_b32 v22, v20\n"
      "s_waitcnt lgkmcnt(0)\n"
      "s_barrier\n"
      "ds_read_b128 v[28:31], v23\n"
      "ds_read_b32 v24, v24\n"
      "s_waitcnt lgkmcnt(0)\n"
      "; first token holding the largest score; its wave publishes the record {score bits | token, tag}\n"
      "v_max3_f32 v20, v28, v29, v30\n"
      "v_max_f32 v20, v20, v31\n"
      "v_cmp_eq_f32 vcc, v24, v20\n"
      "s_ff1_i32_b64 s10, vcc\n"
      "s_max_i32 s10, s10, 0\n"
      "v_or_b32 v20, s10, v20\n"
      "s_lshl_b32 s10, s10, 6\n"
      "s_cmp_lg_u32 s10, s4\n"
      "s_cbranch_scc1 .Lidentity\n"
      "s_mov_b64 exec, 1\n"
      ".Lrecord:\n"
      "s_add_u32 s11, s11, 0x01010101\n"
      "v_mov_b32 v21, s11\n"
      "s_lshl_b32 s70, s70, 18\n"
      "s_lshr_b32 s70, s70, 16\n"
      "v_or_b32 v20, s70, v20   ; bits 2..15: when this block got going, for pacing\n"
      "buffer_store_dwordx2 v[20:21], off, s[24:27], s9 offset:1024 sc1\n"
      ".Lquit:\n"
      "s_endpgm\n"
      "; vote (speed only): two write-through stores; a gap between their completions means that bit 13 = 0 is the nearer die\n"
      ".Lvote:\n"
      "s_cmp_lg_u32 s4, 0\n"
      "s_cbranch_scc1 .Lquit\n"
      "s_lshl_b32 s7, s6, 7\n"
      "s_addk_i32 s7, 0x4000\n"
      "s_xor_b32 s8, s7, 0x2000\n"
      "s_mov_b64 exec, 1\n"
      "buffer_store_dword v3, off, s[24:27], s7 sc1\n"
      "buffer_store_dword v3, off, s[24:27], s8 sc1\n"
      "s_addk_i32 s9, 0x80\n"
      "s_waitcnt vmcnt(1)\n"
      "s_memtime s[62:63]\n"
      "s_waitcnt vmcnt(0)\n"
      "s_memtime s[64:65]\n"
      "s_waitcnt lgkmcnt(0)\n"
      "s_sub_u32 s10, s64, s62\n"
      "s_lshr_b32 s10, s10, 6\n"
      "s_cselect_b32 s10, 1, 0\n"
      "v_mov_b32 v20, s10\n"
      "s_branch .Lrecord\n"
      ".Lworker2:\n"
      "; fragment addresses of phase 1, cleared accumulator\n"
      "v_mov_b32 v40, 0\n"
      "v_mov_b32 v41, 0\n"
      "v_mov_b32 v42, 0\n"
      "v_mov_b32 v43, 0\n"
      "v_lshrrev_b32 v7, 5, v3\n"
      "v_and_b32 v8, 31, v3\n"
      "v_mfma_f32_32x32x16_bf16 v[16:31], v[40:43], v[40:43], 0\n"
      "v_and_b32 v56, 3, v15\n"
      "v_and_b32 v57, 3, v3\n"
      "v_lshlrev_b32 v60, 3, v3\n"
      "v_and_b32 v59, 0x58, v60\n"
      "v_lshlrev_b32 v61, 1, v3\n"
      "v_and_or_b32 v59, v61, 32, v59\n"
      "v_lshl_or_b32 v59, v56, 7, v59\n"
      "v_lshl_or_b32 v12, v7, 10, v59\n"
      "s_lshl_b32 s10, s43, 6\n"
      "s_add_u32 s10, s10, s44\n"
      "v_xor_b32 v12, s10, v12   ; gate/up (transposed reads)\n"
      "v_bfe_u32 v75, v3, 1, 3\n"
      "v_xor_b32 v14, v75, v7\n"
      "v_lshl_add_u32 v14, v14, 4, s47\n"
      "v_lshl_add_u32 v14, v8, 7, v14   ; activations, k-step & 3 = 0..3\n"
      "v_xor_b32 v108, 32, v14\n"
      "v_xor_b32 v109, 64, v14\n"
      "v_xor_b32 v110, 64, v108\n"
      "s_movk_i32 s77, 0x1000\n"
      "s_add_u32 s80, s80, .Lquad-.Lpc\n"
      "; ==== phase 1: acc[weight column][token] += W^T x^T, one 32-column tile per wave, all 32 tokens\n"
      "s_waitcnt vmcnt(6)\n"
      "s_addk_i32 m0, 0x800\n"
      "s_add_u32 s8, s8, s9\n"
      "buffer_load_dwordx4 v1, s[12:15], s8 offen lds\n"
      "s_waitcnt vmcnt(5)\n"
      "s_addk_i32 m0, 0x800\n"
      "s_add_u32 s8, s8, s9\n"
      "buffer_load_dwordx4 v1, s[12:15], s8 offen lds\n"
      "s_waitcnt vmcnt(5)\n"
      "s_addk_i32 m0, 0x800\n"
      "s_add_u32 s8, s8, s9\n"
      "buffer_load_dwordx4 v1, s[12:15], s8 offen lds\n"
      "s_mov_b32 m0, s36\n"
      "s_addk_i32 s36, 0xf80\n"
      "buffer_load_dwordx4 v6, s[16:19], s46 offen offset:256 lds\n"
      "s_add_u32 m0, s37, 0x4000\n"
      "s_add_u32 s8, s8, s9\n"
      "buffer_load_dwordx4 v1, s[12:15], s8 offen lds\n"
      "s_waitcnt vmcnt(7)\n"
      "s_addk_i32 m0, 0x800\n"
      "s_add_u32 s8, s8, s9\n"
      "buffer_load_dwordx4 v1, s[12:15], s8 offen lds\n"
      "s_addk_i32 m0, 0x800\n"
      "s_add_u32 s8, s8, s9\n"
      "buffer_load_dwordx4 v1, s[12:15], s8 offen lds\n"
      "s_waitcnt vmcnt(7)\n"
      "s_addk_i32 m0, 0x800\n"
      "s_add_u32 s8, s8, s9\n"
      "buffer_load_dwordx4 v1, s[12:15], s8 offen lds\n"
      "s_waitcnt vmcnt(7)\n"
      "s_mov_b32 m0, s36\n"
      "s_addk_i32 s36, 0xf80\n"
      "buffer_load_dwordx4 v6, s[16:19], s46 offen offset:384 lds\n"
      "s_add_u32 m0, s37, 0x6000\n"
      "s_add_u32 s8, s8, s9\n"
      "buffer_load_dwordx4 v1, s[12:15], s8 offen lds\n"
      "s_waitcnt vmcnt(8)\n"
      "s_addk_i32 m0, 0x800\n"
      "s_add_u32 s8, s8, s9\n"
      "buffer_load_dwordx4 v1, s[12:15], s8 offen lds\n"
      "; record and w_down source addresses, tag of this call\n"
      "v_lshlrev_b32 v15, 4, v15   ; record lane >> 2\n"
      "v_lshl_or_b32 v10, v4, 11, v5   ; even and odd pieces\n"
      "v_xor_b32 v11, 32, v10\n"
      "s_mov_b32 s42, 0x10000\n"
      "s_lshl_b32 s50, s4, 7\n"
      "s_add_u32 s50, s50, %3\n"
      "s_add_u32 s50, s50, s42   ; slice [64 rows][128 B] of this wave in LDS\n"
      "s_lshl_b32 s42, s42, 4   ; retry budget\n"
      "s_lshl_b32 s51, s6, 17\n"
      "s_add_u32 s51, s51, s45\n"
      "s_lshl1_add_u32 s51, s4, s51\n"
      "s_waitcnt lgkmcnt(0)\n"
      "s_add_u32 s11, s11, 0x01010101\n"
      "v_mov_b32 v67, s11\n"
      "s_swappc_b64 s[78:79], s[80:81]\n"
      "s_waitcnt vmcnt(8)\n"
      "s_addk_i32 m0, 0x800\n"
      "s_add_u32 s8, s8, s9\n"
      "buffer_load_dwordx4 v1, s[12:15], s8 offen lds\n"
      "s_waitcnt vmcnt(7)\n"
      "s_addk_i32 m0, 0x800\n"
      "s_add_u32 s8, s8, s9\n"
      "buffer_load_dwordx4 v1, s[12:15], s8 offen lds\n"
      "s_waitcnt vmcnt(7)\n"
      "; hand-off buffers: address bit 13 follows bit 2 of the reading group, so that a line lies on the die of its readers\n"
      "s_and_b32 s86, s6, 4\n"
      "s_lshl_b32 s86, s86, 11\n"
      "s_and_b32 s87, s7, 2\n"
      "s_add_u32 s87, s87, s7\n"
      "s_lshl_b32 s87, s87, 12\n"
      "v_and_b32 v35, 0xe0, v60\n"
      "v_lshl_or_b32 v35, v7, 4, v35   ; hand-off 1 store\n"
      "s_lshr_b32 s52, s85, 3\n"
      "s_and_b32 s10, s85, 3\n"
      "s_lshl2_add_u32 s52, s52, s10\n"
      "s_lshl_b32 s52, s52, 15\n"
      "s_add_u32 s52, s52, s86\n"
      "s_add_u32 s52, s52, s87\n"
      "s_lshl1_add_u32 s10, s84, s43\n"
      "s_add_u32 s10, s10, 32\n"
      "s_lshl_b32 s10, s10, 10\n"
      "s_add_u32 s52, s52, s10\n"
      "s_mov_b32 s38, 0x07060500   ; byte selectors that put tag byte i under a value\n"
      "s_add_u32 s39, s38, 1\n"
      "s_add_u32 s40, s38, 2\n"
      "s_add_u32 s41, s38, 3\n"
      "s_waitcnt vmcnt(6)\n"
      "buffer_load_dwordx2 v[68:69], v15, s[24:27], 0 offen offset:1024 sc1\n"
      "s_swappc_b64 s[78:79], s[80:81]\n"
      "s_waitcnt vmcnt(6)\n"
      "s_mov_b32 m0, s50\n"
      "s_nop 0\n"
      "buffer_load_dwordx4 v10, s[20:23], s51 offen lds\n"
      "s_addk_i32 m0, 0x400\n"
      "s_addk_i32 s51, 0x4000\n"
      "buffer_load_dwordx4 v11, s[20:23], s51 offen lds\n"
      "s_addk_i32 m0, 0x400\n"
      "s_addk_i32 s51, 0x4000\n"
      "buffer_load_dwordx4 v10, s[20:23], s51 offen lds\n"
      "s_addk_i32 m0, 0x400\n"
      "s_addk_i32 s51, 0x4000\n"
      "buffer_load_dwordx4 v11, s[20:23], s51 offen lds\n"
      "s_swappc_b64 s[78:79], s[80:81]\n"
      "s_waitcnt vmcnt(8)\n"
      "s_addk_i32 m0, 0x400\n"
      "s_addk_i32 s51, 0x4000\n"
      "buffer_load_dwordx4 v10, s[20:23], s51 offen lds\n"
      "s_addk_i32 m0, 0x400\n"
      "s_addk_i32 s51, 0x4000\n"
      "buffer_load_dwordx4 v11, s[20:23], s51 offen lds\n"
      "s_addk_i32 m0, 0x400\n"
      "s_addk_i32 s51, 0x4000\n"
      "buffer_load_dwordx4 v10, s[20:23], s51 offen lds\n"
      "s_addk_i32 m0, 0x400\n"
      "s_addk_i32 s51, 0x4000\n"
      "buffer_load_dwordx4 v11, s[20:23], s51 offen lds\n"
      "s_barrier\n"
      "ds_read_b64_tr_b16 v[76:77], v12 offset:0\n"
      "ds_read_b64_tr_b16 v[78:79], v12 offset:512\n"
      "ds_read_b128 v[80:83], v14\n"
      "s_waitcnt vmcnt(11)\n"
      "s_barrier\n"
      "ds_read_b64_tr_b16 v[84:85], v12 offset:2048\n"
      "ds_read_b64_tr_b16 v[86:87], v12 offset:2560\n"
      "ds_read_b128 v[88:91], v108\n"
      "s_waitcnt lgkmcnt(3)\n"
      "v_mfma_f32_32x32x16_bf16 v[16:31], v[76:79], v[80:83], v[16:31]\n"
      "s_waitcnt vmcnt(10)\n"
      "s_barrier\n"
      "ds_read_b64_tr_b16 v[92:93], v12 offset:4096\n"
      "ds_read_b64_tr_b16 v[94:95], v12 offset:4608\n"
      "ds_read_b128 v[96:99], v109\n"
      "s_waitcnt lgkmcnt(3)\n"
      "v_mfma_f32_32x32x16_bf16 v[16:31], v[84:87], v[88:91], v[16:31]\n"
      "ds_read_b128 v[104:107], v110\n"
      "s_waitcnt lgkmcnt(1)\n"
      "v_mfma_f32_32x32x16_bf16 v[16:31], v[92:95], v[96:99], v[16:31]\n"
      "s_waitcnt vmcnt(8)\n"
      "s_barrier\n"
      "ds_read_b64_tr_b16 v[100:101], v12 offset:6144\n"
      "ds_read_b64_tr_b16 v[102:103], v12 offset:6656\n"
      "; records: lanes 0-31 hold the routes of the 8 sequences, lanes 32-63 the 8 votes on address bit 13\n"
      ".Lrec:\n"
      "v_cmp_ne_u32 vcc, s11, v69\n"
      "s_cbranch_vccnz .Lrec_retry\n"
      "v_and_b32 v70, 3, v68\n"
      "v_cmp_eq_u32 s[48:49], v70, v57\n"
      "v_cmp_ne_u32 vcc, 0, v70\n"
      "s_bcnt1_i32_b32 s10, vcc_hi\n"
      "s_cmp_gt_u32 s10, 16\n"
      "s_cselect_b32 s88, 0x2000, 0\n"
      "s_xor_b32 s52, s52, s88\n"
      "s_mov_b32 s49, s48\n"
      "s_waitcnt lgkmcnt(0)\n"
      "v_mfma_f32_32x32x16_bf16 v[16:31], v[100:103], v[104:107], v[16:31]\n"
      "; ==== hand-off 1, publish: the lanes of the routed tokens store tagged partial sums\n"
      "; (twelve scalar instructions: the wait states between the last MFMA and its first reader)\n"
      "s_mov_b64 exec, s[48:49]\n"
      "s_xor_b32 s86, s86, s88\n"
      "s_lshr_b32 s53, s6, 3\n"
      "s_and_b32 s10, s6, 3\n"
      "s_lshl2_add_u32 s53, s53, s10\n"
      "s_lshl_b32 s53, s53, 15\n"
      "s_add_u32 s53, s53, s86\n"
      "s_lshl3_add_u32 s53, s4, s53\n"
      "s_lshl3_add_u32 s53, s77, s53\n"
      "s_add_u32 s54, s53, s77\n"
      "s_lshl2_add_u32 s55, s77, s53\n"
      "s_add_u32 s56, s55, s77\n"
      "v_perm_b32 v16, v16, v67, s38\n"
      "v_perm_b32 v17, v17, v67, s39\n"
      "v_perm_b32 v18, v18, v67, s40\n"
      "v_perm_b32 v19, v19, v67, s41\n"
      "buffer_store_dwordx4 v[16:19], v35, s[24:27], s52 offen offset:0 sc1\n"
      "v_perm_b32 v20, v20, v67, s38\n"
      "v_perm_b32 v21, v21, v67, s39\n"
      "v_perm_b32 v22, v22, v67, s40\n"
      "v_perm_b32 v23, v23, v67, s41\n"
      "buffer_store_dwordx4 v[20:23], v35, s[24:27], s52 offen offset:256 sc1\n"
      "v_perm_b32 v24, v24, v67, s38\n"
      "v_perm_b32 v25, v25, v67, s39\n"
      "v_perm_b32 v26, v26, v67, s40\n"
      "v_perm_b32 v27, v27, v67, s41\n"
      "buffer_store_dwordx4 v[24:27], v35, s[24:27], s52 offen offset:512 sc1\n"
      "v_perm_b32 v28, v28, v67, s38\n"
      "v_perm_b32 v29, v29, v67, s39\n"
      "v_perm_b32 v30, v30, v67, s40\n"
      "v_perm_b32 v31, v31, v67, s41\n"
      "buffer_store_dwordx4 v[28:31], v35, s[24:27], s52 offen offset:768 sc1\n"
      "s_mov_b64 exec, -1\n"
      "; addresses of the phases behind the first hand-off\n"
      "v_lshlrev_b32 v36, 4, v8\n"
      "v_lshl_or_b32 v36, v7, 11, v36   ; hand-off 1 load\n"
      "s_movk_i32 s60, 0xff\n"
      "s_and_b32 s64, s11, s60\n"
      "s_bfe_u32 s65, s11, 0x80008\n"
      "s_bfe_u32 s66, s11, 0x80010\n"
      "s_lshr_b32 s67, s11, 24\n"
      "v_lshl_or_b32 v13, v58, 10, v59\n"
      "v_add_u32 v13, s50, v13   ; w_down fragments, column tiles 0..3\n"
      "v_xor_b32 v32, 32, v13\n"
      "v_xor_b32 v33, 64, v13\n"
      "v_xor_b32 v34, 64, v32\n"
      "s_movk_i32 s77, 144\n"
      "v_and_b32 v61, 16, v3\n"
      "v_and_or_b32 v61, v60, 8, v61\n"
      "v_lshl_or_b32 v61, v7, 2, v61\n"
      "v_mad_u32_u24 v37, v75, s77, v61\n"
      "s_lshr_b32 s10, s4, 1\n"
      "s_add_u32 s57, %3, s9\n"
      "s_add_u32 s10, s10, s57\n"
      "v_add_u32 v37, s10, v37   ; h write\n"
      "v_lshl_add_u32 v61, v58, 4, s57\n"
      "v_mad_u32_u24 v61, v62, s77, v61\n"
      "s_addk_i32 s57, 0x480\n"
      "v_and_b32 v60, 64, v60\n"
      "v_mov_b32 v63, s57\n"
      "v_cmp_eq_u32 vcc, 0, v60\n"
      "v_lshl_add_u32 v64, v8, 2, v63\n"
      "v_lshl_or_b32 v66, v57, 7, v60\n"
      "v_cndmask_b32 v38, v63, v61, vcc   ; h fragments for lanes with bit 3 clear, zeros elsewhere\n"
      "v_cndmask_b32 v39, v61, v63, vcc   ; and the reverse\n"
      "ds_write_b32 v64, v40\n"
      "v_and_b32 v61, 4, v3\n"
      "v_lshl_or_b32 v66, v61, 11, v66\n"
      "v_lshl_or_b32 v66, v58, 4, v66\n"
      "v_xor_b32 v66, s88, v66   ; hand-off 2 store\n"
      "s_add_u32 s59, s87, 0x400000\n"
      "s_lshl_b32 s58, s6, 15\n"
      "s_add_u32 s58, s58, s59\n"
      "s_lshl4_add_u32 s58, s4, s58\n"
      "; final-sum task t = g + 56 * wave (t < 64): sequence t & 7, 32-channel block t >> 3 of channels [256j, 256j + 256)\n"
      "s_lshr_b32 s10, s4, 6\n"
      "s_mul_i32 s10, s10, 56\n"
      "s_add_u32 s61, s6, s10\n"
      "s_sleep 3\n"
      "; pacing only: a read ahead of its writer is slow, so wait (bounded) until the stream of the XCD that started last can be over\n"
      "v_lshlrev_b32 v65, 16, v68\n"
      "v_add_u32 v65, 0x58c0000, v65\n"
      "s_movk_i32 s89, 32\n"
      ".Lcg1:\n"
      "s_memrealtime s[90:91]\n"
      "s_sub_u32 s89, s89, 1\n"
      "s_waitcnt lgkmcnt(0)\n"
      "s_cbranch_scc1 .Lcg1_x\n"
      "s_lshl_b32 s90, s90, 18\n"
      "v_sub_u32 v70, s90, v65\n"
      "v_cmp_gt_i32 vcc, 0, v70\n"
      "s_cbranch_vccnz .Lcg1\n"
      ".Lcg1_x:\n"
      "; ==== hand-off 1, gather: lanes 0-31 gate chunks, lanes 32-63 the matching up chunks, one load per K quarter\n"
      "buffer_load_dwordx4 v[16:19], v36, s[24:27], s53 offen sc1\n"
      "buffer_load_dwordx4 v[20:23], v36, s[24:27], s54 offen sc1\n"
      "buffer_load_dwordx4 v[24:27], v36, s[24:27], s55 offen sc1\n"
      "buffer_load_dwordx4 v[28:31], v36, s[24:27], s56 offen sc1\n"
      "s_waitcnt vmcnt(12)\n"
      "ds_read_b64_tr_b16 v[76:77], v13 offset:0\n"
      "ds_read_b64_tr_b16 v[78:79], v13 offset:512\n"
      "ds_read_b64_tr_b16 v[84:85], v32 offset:0\n"
      "ds_read_b64_tr_b16 v[86:87], v32 offset:512\n"
      "ds_read_b64_tr_b16 v[92:93], v33 offset:0\n"
      "ds_read_b64_tr_b16 v[94:95], v33 offset:512\n"
      "ds_read_b64_tr_b16 v[100:101], v34 offset:0\n"
      "ds_read_b64_tr_b16 v[102:103], v34 offset:512\n"
      "s_waitcnt vmcnt(8)\n"
      "ds_read_b64_tr_b16 v[80:81], v13 offset:4096\n"
      "ds_read_b64_tr_b16 v[82:83], v13 offset:4608\n"
      "ds_read_b64_tr_b16 v[88:89], v32 offset:4096\n"
      "ds_read_b64_tr_b16 v[90:91], v32 offset:4608\n"
      "ds_read_b64_tr_b16 v[96:97], v33 offset:4096\n"
      "ds_read_b64_tr_b16 v[98:99], v33 offset:4608\n"
      "ds_read_b64_tr_b16 v[104:105], v34 offset:4096\n"
      "ds_read_b64_tr_b16 v[106:107], v34 offset:4608\n"
      "; XOR with the tag clears the low byte of a fresh dword; any other low byte marks a stale chunk\n"
      ".Lg1:\n"
      "s_waitcnt vmcnt(3)\n"
      "v_xor_b32 v16, s64, v16\n"
      "v_xor_b32 v17, s65, v17\n"
      "v_xor_b32 v18, s66, v18\n"
      "v_xor_b32 v19, s67, v19\n"
      "v_or3_b32 v9, v16, v17, v18\n"
      "s_waitcnt vmcnt(2)\n"
      "v_xor_b32 v20, s64, v20\n"
      "v_xor_b32 v21, s65, v21\n"
      "v_xor_b32 v22, s66, v22\n"
      "v_xor_b32 v23, s67, v23\n"
      "v_or3_b32 v9, v9, v19, v20\n"
      "v_pk_add_f32 v[16:17], v[16:17], v[20:21]\n"
      "v_pk_add_f32 v[18:19], v[18:19], v[22:23]\n"
      "v_or3_b32 v9, v9, v21, v22\n"
      "s_waitcnt vmcnt(1)\n"
      "v_xor_b32 v24, s64, v24\n"
      "v_xor_b32 v25, s65, v25\n"
      "v_xor_b32 v26, s66, v26\n"
      "v_xor_b32 v27, s67, v27\n"
      "v_or3_b32 v9, v9, v23, v24\n"
      "v_pk_add_f32 v[16:17], v[16:17], v[24:25]\n"
      "v_pk_add_f32 v[18:19], v[18:19], v[26:27]\n"
      "v_or3_b32 v9, v9, v25, v26\n"
      "s_waitcnt vmcnt(0)\n"
      "v_xor_b32 v28, s64, v28\n"
      "v_xor_b32 v29, s65, v29\n"
      "v_xor_b32 v30, s66, v30\n"
      "v_xor_b32 v31, s67, v31\n"
      "v_or3_b32 v9, v9, v27, v28\n"
      "v_pk_add_f32 v[16:17], v[16:17], v[28:29]\n"
      "v_pk_add_f32 v[18:19], v[18:19], v[30:31]\n"
      "v_or3_b32 v9, v9, v29, v30\n"
      "v_bitop3_b32 v9, v9, s60, v31 bitop3:0xc8\n"
      "v_cmp_ne_u32 vcc, 0, v9\n"
      "s_mov_b64 exec, -1\n"
      "s_cbranch_vccnz .Lg1_retry\n"
      "v_permlane32_swap_b32 v16, v18\n"
      "v_permlane32_swap_b32 v17, v19\n"
      "; h = bf16(bf16(silu(bf16 g)) * bf16 u), two elements per lane\n"
      "v_cvt_pk_bf16_f32 v16, 0, v16\n"
      "v_cvt_pk_bf16_f32 v17, 0, v17\n"
      "v_mul_f32 v20, 0xbfb8aa3b, v16\n"
      "v_mul_f32 v21, 0xbfb8aa3b, v17\n"
      "v_exp_f32 v20, v20\n"
      "v_exp_f32 v21, v21\n"
      "v_cvt_pk_bf16_f32 v18, 0, v18\n"
      "v_pk_add_f32 v[20:21], v[20:21], 1.0 op_sel_hi:[1,0]\n"
      "v_rcp_f32 v20, v20\n"
      "v_rcp_f32 v21, v21\n"
      "v_cvt_pk_bf16_f32 v19, 0, v19\n"
      "v_pk_mul_f32 v[16:17], v[16:17], v[20:21]\n"
      "v_cvt_pk_bf16_f32 v16, 0, v16\n"
      "v_cvt_pk_bf16_f32 v17, 0, v17\n"
      "v_pk_mul_f32 v[16:17], v[16:17], v[18:19]\n"
      "v_cvt_pk_bf16_f32 v16, v16, v17\n"
      "ds_write_b32 v37, v16\n"
      "s_waitcnt lgkmcnt(0)\n"
      "s_barrier\n"
      "; ==== phase 2: accumulator a holds column tile 2a in lanes with bit 3 clear and tile 2a + 1 in the others\n"
      "ds_read_b128 v[40:43], v38\n"
      "ds_read_b128 v[44:47], v38 offset:64\n"
      "ds_read_b128 v[48:51], v39\n"
      "ds_read_b128 v[52:55], v39 offset:64\n"
      "s_waitcnt lgkmcnt(3)\n"
      "v_mfma_f32_16x16x32_bf16 v[16:19], v[76:79], v[40:43], 0\n"
      "v_mfma_f32_16x16x32_bf16 v[20:23], v[92:95], v[40:43], 0\n"
      "s_waitcnt lgkmcnt(2)\n"
      "v_mfma_f32_16x16x32_bf16 v[16:19], v[80:83], v[44:47], v[16:19]\n"
      "v_mfma_f32_16x16x32_bf16 v[20:23], v[96:99], v[44:47], v[20:23]\n"
      "s_waitcnt lgkmcnt(1)\n"
      "v_mfma_f32_16x16x32_bf16 v[16:19], v[84:87], v[48:51], v[16:19]\n"
      "v_mfma_f32_16x16x32_bf16 v[20:23], v[100:103], v[48:51], v[20:23]\n"
      "s_waitcnt lgkmcnt(0)\n"
      "v_mfma_f32_16x16x32_bf16 v[16:19], v[88:91], v[52:55], v[16:19]\n"
      "v_mfma_f32_16x16x32_bf16 v[20:23], v[104:107], v[52:55], v[20:23]\n"
      "; (seven scalar instructions: wait states)\n"
      "s_lshr_b32 s70, s61, 3\n"
      "s_and_b32 s10, s61, 3\n"
      "s_lshl2_add_u32 s70, s70, s10\n"
      "s_lshl_b32 s70, s70, 7\n"
      "s_add_u32 s70, s70, s86\n"
      "s_add_u32 s70, s70, s59\n"
      "s_cmp_lt_u32 s61, 64\n"
      "v_perm_b32 v16, v16, v67, s38\n"
      "v_perm_b32 v17, v17, v67, s39\n"
      "v_perm_b32 v18, v18, v67, s40\n"
      "v_perm_b32 v19, v19, v67, s41\n"
      "buffer_store_dwordx4 v[16:19], v66, s[24:27], s58 offen offset:0 sc1\n"
      "v_perm_b32 v20, v20, v67, s38\n"
      "v_perm_b32 v21, v21, v67, s39\n"
      "v_perm_b32 v22, v22, v67, s40\n"
      "v_perm_b32 v23, v23, v67, s41\n"
      "buffer_store_dwordx4 v[20:23], v66, s[24:27], s58 offen offset:512 sc1\n"
      "s_cbranch_scc0 .Lexit\n"
      "; ==== hand-off 2: 56 partials of (sequence, 32 channels); lane = (group & 7, 4-channel chunk), load = group >> 3\n"
      "s_lshl_b32 s10, s83, 1\n"
      "s_add_u32 s71, s70, s10\n"
      "s_add_u32 s72, s71, s10\n"
      "s_add_u32 s73, s72, s10\n"
      "s_add_u32 s74, s73, s10\n"
      "s_add_u32 s75, s74, s10\n"
      "s_add_u32 s76, s75, s10\n"
      "v_lshlrev_b32 v71, 4, v62\n"
      "v_lshl_or_b32 v71, v4, 15, v71   ; hand-off 2 load\n"
      "v_lshlrev_b32 v74, 3, v62\n"
      "v_lshl_or_b32 v74, v58, 1, v74   ; output element\n"
      "; router weight bf16(sigmoid(r)) and residual x + x * 0 of the task\n"
      "s_and_b32 s62, s6, 7\n"
      "s_lshl_b32 s62, s62, 2\n"
      "v_readlane_b32 s63, v68, s62\n"
      "v_mov_b32 v72, 0xbfb8aa3b\n"
      "s_lshr_b32 s68, s61, 3\n"
      "s_and_b32 s10, s63, 3\n"
      "s_add_u32 s62, s62, s10   ; routed token\n"
      "s_and_b32 s63, s63, 0xffff0000\n"
      "v_mul_f32 v72, s63, v72\n"
      "v_exp_f32 v72, v72\n"
      "s_lshl_b32 s69, s68, 6\n"
      "v_add_f32 v72, 1.0, v72\n"
      "v_rcp_f32 v72, v72\n"
      "s_lshl_b32 s10, s62, 3\n"
      "v_cvt_pk_bf16_f32 v72, 0, v72\n"
      "s_xor_b32 s10, s10, s69\n"
      "s_and_b32 s10, s10, 0x70\n"
      "s_add_u32 s10, s10, s47\n"
      "s_lshr_b32 s63, s68, 1\n"
      "s_lshl_b32 s63, s63, 5\n"
      "s_add_u32 s63, s63, s62\n"
      "s_lshl_b32 s63, s63, 7\n"
      "s_add_u32 s10, s10, s63\n"
      "v_xor_b32 v73, s10, v74\n"
      "ds_read_u16 v73, v73\n"
      "s_add_u32 s69, s69, s45\n"
      "s_lshl_b32 s10, s62, 11\n"
      "s_add_u32 s69, s69, s10\n"
      "s_waitcnt lgkmcnt(0)\n"
      "v_lshlrev_b32 v73, 16, v73\n"
      "v_fmac_f32 v73, 0, v73\n"
      "s_sleep 15\n"
      ".Lg2:\n"
      "buffer_load_dwordx4 v[76:79], v71, s[24:27], s70 offen sc1\n"
      "buffer_load_dwordx4 v[80:83], v71, s[24:27], s71 offen sc1\n"
      "buffer_load_dwordx4 v[84:87], v71, s[24:27], s72 offen sc1\n"
      "buffer_load_dwordx4 v[88:91], v71, s[24:27], s73 offen sc1\n"
      "buffer_load_dwordx4 v[92:95], v71, s[24:27], s74 offen sc1\n"
      "buffer_load_dwordx4 v[96:99], v71, s[24:27], s75 offen sc1\n"
      "buffer_load_dwordx4 v[100:103], v71, s[24:27], s76 offen sc1\n"
      "s_waitcnt vmcnt(6)\n"
      "v_xor_b32 v76, s64, v76\n"
      "v_xor_b32 v77, s65, v77\n"
      "v_xor_b32 v78, s66, v78\n"
      "v_xor_b32 v79, s67, v79\n"
      "v_or3_b32 v9, v76, v77, v78\n"
      "s_waitcnt vmcnt(5)\n"
      "v_xor_b32 v80, s64, v80\n"
      "v_xor_b32 v81, s65, v81\n"
      "v_xor_b32 v82, s66, v82\n"
      "v_xor_b32 v83, s67, v83\n"
      "v_or3_b32 v9, v9, v79, v80\n"
      "v_pk_add_f32 v[76:77], v[76:77], v[80:81]\n"
      "v_pk_add_f32 v[78:79], v[78:79], v[82:83]\n"
      "v_or3_b32 v9, v9, v81, v82\n"
      "s_waitcnt vmcnt(4)\n"
      "v_xor_b32 v84, s64, v84\n"
      "v_xor_b32 v85, s65, v85\n"
      "v_xor_b32 v86, s66, v86\n"
      "v_xor_b32 v87, s67, v87\n"
      "v_or3_b32 v9, v9, v83, v84\n"
      "v_pk_add_f32 v[76:77], v[76:77], v[84:85]\n"
      "v_pk_add_f32 v[78:79], v[78:79], v[86:87]\n"
      "v_or3_b32 v9, v9, v85, v86\n"
      "s_waitcnt vmcnt(3)\n"
      "v_xor_b32 v88, s64, v88\n"
      "v_xor_b32 v89, s65, v89\n"
      "v_xor_b32 v90, s66, v90\n"
      "v_xor_b32 v91, s67, v91\n"
      "v_or3_b32 v9, v9, v87, v88\n"
      "v_pk_add_f32 v[76:77], v[76:77], v[88:89]\n"
      "v_pk_add_f32 v[78:79], v[78:79], v[90:91]\n"
      "v_or3_b32 v9, v9, v89, v90\n"
      "s_waitcnt vmcnt(2)\n"
      "v_xor_b32 v92, s64, v92\n"
      "v_xor_b32 v93, s65, v93\n"
      "v_xor_b32 v94, s66, v94\n"
      "v_xor_b32 v95, s67, v95\n"
      "v_or3_b32 v9, v9, v91, v92\n"
      "v_pk_add_f32 v[76:77], v[76:77], v[92:93]\n"
      "v_pk_add_f32 v[78:79], v[78:79], v[94:95]\n"
      "v_or3_b32 v9, v9, v93, v94\n"
      "s_waitcnt vmcnt(1)\n"
      "v_xor_b32 v96, s64, v96\n"
      "v_xor_b32 v97, s65, v97\n"
      "v_xor_b32 v98, s66, v98\n"
      "v_xor_b32 v99, s67, v99\n"
      "v_or3_b32 v9, v9, v95, v96\n"
      "v_pk_add_f32 v[76:77], v[76:77], v[96:97]\n"
      "v_pk_add_f32 v[78:79], v[78:79], v[98:99]\n"
      "v_or3_b32 v9, v9, v97, v98\n"
      "s_waitcnt vmcnt(0)\n"
      "v_xor_b32 v100, s64, v100\n"
      "v_xor_b32 v101, s65, v101\n"
      "v_xor_b32 v102, s66, v102\n"
      "v_xor_b32 v103, s67, v103\n"
      "v_or3_b32 v9, v9, v99, v100\n"
      "v_pk_add_f32 v[76:77], v[76:77], v[100:101]\n"
      "v_pk_add_f32 v[78:79], v[78:79], v[102:103]\n"
      "v_or3_b32 v9, v9, v101, v102\n"
      "v_bitop3_b32 v9, v9, s60, v103 bitop3:0xc8\n"
      "v_cmp_ne_u32 vcc, 0, v9\n"
      "s_mov_b64 exec, -1\n"
      "s_cbranch_vccnz .Lg2_retry\n"
      "; sum over the eight lane groups; row r of the wave ends with element r of every chunk\n"
      "v_permlane32_swap_b32 v76, v78\n"
      "v_permlane32_swap_b32 v77, v79\n"
      "v_pk_add_f32 v[76:77], v[76:77], v[78:79]\n"
      "s_nop 1\n"
      "v_permlane16_swap_b32 v76, v77\n"
      "v_add_f32 v76, v76, v77\n"
      "s_or_b32 s10, s61, s7\n"
      "s_nop 0\n"
      "v_add_f32_dpp v76, v76, v76 row_ror:8 row_mask:0xf bank_mask:0xf\n"
      "; out = bf16(x + bf16(bf16(y) * router weight))\n"
      "v_cvt_pk_bf16_f32 v76, 0, v76\n"
      "v_mul_f32 v76, v76, v72\n"
      "v_cvt_pk_bf16_f32 v76, 0, v76\n"
      "v_add_f32 v76, v73, v76\n"
      "v_cvt_pk_bf16_f32 v76, v76, v76\n"
      "buffer_store_short v76, v74, s[28:31], s69 offen\n"
      "s_cbranch_scc1 .Lexit\n"
      "; task 0 of j = 0: its sum depends on every workgroup, so all of them have read the epoch word: advance it\n"
      "s_mov_b64 exec, 1\n"
      "buffer_store_dword v67, off, s[24:27], 0 sc1\n"
      ".Lexit:\n"
      "s_endpgm\n"
      "; ==== retries: quads of lanes that saw a stale chunk read again; a wave that exhausts its budget ends\n"
      ".Lg2_retry:\n"
      "s_sub_u32 s42, s42, 1\n"
      "s_cbranch_scc1 .Lexit\n"
      "s_wqm_b64 exec, vcc\n"
      "s_branch .Lg2\n"
      ".Lg1_retry:\n"
      "s_sub_u32 s42, s42, 1\n"
      "s_cbranch_scc1 .Lexit\n"
      "s_wqm_b64 exec, vcc\n"
      "buffer_load_dwordx4 v[16:19], v36, s[24:27], s53 offen sc1\n"
      "buffer_load_dwordx4 v[20:23], v36, s[24:27], s54 offen sc1\n"
      "buffer_load_dwordx4 v[24:27], v36, s[24:27], s55 offen sc1\n"
      "buffer_load_dwordx4 v[28:31], v36, s[24:27], s56 offen sc1\n"
      "s_branch .Lg1\n"
      ".Lrec_retry:\n"
      "s_sub_u32 s42, s42, 1\n"
      "s_cbranch_scc1 .Lexit\n"
      "buffer_load_dwordx2 v[68:69], v15, s[24:27], 0 offen offset:1024 sc1\n"
      "s_waitcnt vmcnt(0)\n"
      "s_branch .Lrec\n"
      "; four k-steps whose pieces have landed in all waves; advances the fragment addresses\n"
      ".Lquad:\n"
      "s_barrier\n"
      "ds_read_b64_tr_b16 v[76:77], v12 offset:0\n"
      "ds_read_b64_tr_b16 v[78:79], v12 offset:512\n"
      "ds_read_b128 v[80:83], v14\n"
      "ds_read_b64_tr_b16 v[84:85], v12 offset:2048\n"
      "ds_read_b64_tr_b16 v[86:87], v12 offset:2560\n"
      "ds_read_b128 v[88:91], v108\n"
      "ds_read_b64_tr_b16 v[92:93], v12 offset:4096\n"
      "ds_read_b64_tr_b16 v[94:95], v12 offset:4608\n"
      "ds_read_b128 v[96:99], v109\n"
      "ds_read_b64_tr_b16 v[100:101], v12 offset:6144\n"
      "ds_read_b64_tr_b16 v[102:103], v12 offset:6656\n"
      "ds_read_b128 v[104:107], v110\n"
      "v_add_u32 v12, 0x2000, v12\n"
      "v_add_u32 v14, s77, v14\n"
      "v_add_u32 v108, s77, v108\n"
      "v_add_u32 v109, s77, v109\n"
      "v_add_u32 v110, s77, v110\n"
      "s_waitcnt lgkmcnt(9)\n"
      "v_mfma_f32_32x32x16_bf16 v[16:31], v[76:79], v[80:83], v[16:31]\n"
      "s_waitcnt lgkmcnt(6)\n"
      "v_mfma_f32_32x32x16_bf16 v[16:31], v[84:87], v[88:91], v[16:31]\n"
      "s_waitcnt lgkmcnt(3)\n"
      "v_mfma_f32_32x32x16_bf16 v[16:31], v[92:95], v[96:99], v[16:31]\n"
      "s_waitcnt lgkmcnt(0)\n"
      "v_mfma_f32_32x32x16_bf16 v[16:31], v[100:103], v[104:107], v[16:31]\n"
      "s_setpc_b64 s[78:79]\n"
      ".Lhot_end:\n"
      "; ==== tokens that are not routed: x + x * 0\n"
      ".Lidentity:\n"
      "v_lshlrev_b32 v24, 16, v4\n"
      "v_and_b32 v25, 0xffff0000, v4\n"
      "v_pk_fma_f32 v[24:25], v[24:25], 0, v[24:25] op_sel_hi:[1,0,1]\n"
      "v_cvt_pk_bf16_f32 v4, v24, v25\n"
      "v_lshlrev_b32 v24, 16, v5\n"
      "v_and_b32 v25, 0xffff0000, v5\n"
      "v_pk_fma_f32 v[24:25], v[24:25], 0, v[24:25] op_sel_hi:[1,0,1]\n"
      "v_cvt_pk_bf16_f32 v5, v24, v25\n"
      "v_lshlrev_b32 v24, 16, v6\n"
      "v_and_b32 v25, 0xffff0000, v6\n"
      "v_pk_fma_f32 v[24:25], v[24:25], 0, v[24:25] op_sel_hi:[1,0,1]\n"
      "v_cvt_pk_bf16_f32 v6, v24, v25\n"
      "v_lshlrev_b32 v24, 16, v7\n"
      "v_and_b32 v25, 0xffff0000, v7\n"
      "v_pk_fma_f32 v[24:25], v[24:25], 0, v[24:25] op_sel_hi:[1,0,1]\n"
      "v_cvt_pk_bf16_f32 v7, v24, v25\n"
      "buffer_store_dwordx4 v[4:7], v1, s[28:31], s8 offen offset:0\n"
      "v_lshlrev_b32 v24, 16, v8\n"
      "v_and_b32 v25, 0xffff0000, v8\n"
      "v_pk_fma_f32 v[24:25], v[24:25], 0, v[24:25] op_sel_hi:[1,0,1]\n"
      "v_cvt_pk_bf16_f32 v8, v24, v25\n"
      "v_lshlrev_b32 v24, 16, v9\n"
      "v_and_b32 v25, 0xffff0000, v9\n"
      "v_pk_fma_f32 v[24:25], v[24:25], 0, v[24:25] op_sel_hi:[1,0,1]\n"
      "v_cvt_pk_bf16_f32 v9, v24, v25\n"
      "v_lshlrev_b32 v24, 16, v10\n"
      "v_and_b32 v25, 0xffff0000, v10\n"
      "v_pk_fma_f32 v[24:25], v[24:25], 0, v[24:25] op_sel_hi:[1,0,1]\n"
      "v_cvt_pk_bf16_f32 v10, v24, v25\n"
      "v_lshlrev_b32 v24, 16, v11\n"
      "v_and_b32 v25, 0xffff0000, v11\n"
      "v_pk_fma_f32 v[24:25], v[24:25], 0, v[24:25] op_sel_hi:[1,0,1]\n"
      "v_cvt_pk_bf16_f32 v11, v24, v25\n"
      "buffer_store_dwordx4 v[8:11], v1, s[28:31], s8 offen offset:1024\n"
      "s_endpgm\n"
      :
      : "s"(__builtin_amdgcn_kernarg_segment_ptr()), "s"(blockIdx.x), "v"(threadIdx.x),
        "s"((unsigned)(size_t)(__attribute__((address_space(3))) char*)smem)
      : "v1", "v2", "v3", "v4", "v5", "v6", "v7", "v8", "v9", "v10", "v11", "v12", "v13", "v14", "v15", "v16",
        "v17", "v18", "v19", "v20", "v21", "v22", "v23", "v24", "v25", "v26", "v27", "v28", "v29", "v30", "v31", "v32",
        "v33", "v34", "v35", "v36", "v37", "v38", "v39", "v40", "v41", "v42", "v43", "v44", "v45", "v46", "v47", "v48",
        "v49", "v50", "v51", "v52", "v53", "v54", "v55", "v56", "v57", "v58", "v59", "v60", "v61", "v62", "v63", "v64",
        "v65", "v66", "v67", "v68", "v69", "v70", "v71", "v72", "v73", "v74", "v75", "v76", "v77", "v78", "v79", "v80",
        "v81", "v82", "v83", "v84", "v85", "v86", "v87", "v88", "v89", "v90", "v91", "v92", "v93", "v94", "v95", "v96",
        "v97", "v98", "v99", "v100", "v101", "v102", "v103", "v104", "v105", "v106", "v107", "v108", "v109", "v110", "v111", "v112",
        "v113", "v114", "v115", "v116", "v117", "v118", "v119", "v120", "v121", "v122", "v123", "v124", "v125", "v126", "v127", "s4",
        "s5", "s6", "s7", "s8", "s9", "s10", "s11", "s12", "s13", "s14", "s15", "s16", "s17", "s18", "s19", "s20",
        "s21", "s22", "s23", "s24", "s25", "s26", "s27", "s28", "s29", "s30", "s31", "s36", "s37", "s38", "s39", "s40",
        "s41", "s42", "s43", "s44", "s45", "s46", "s47", "s48", "s49", "s50", "s51", "s52", "s53", "s54", "s55", "s56",
        "s57", "s58", "s59", "s60", "s61", "s62", "s63", "s64", "s65", "s66", "s67", "s68", "s69", "s70", "s71", "s72",
        "s73", "s74", "s75", "s76", "s77", "s78", "s79", "s80", "s81", "s82", "s83", "s84", "s85", "s86", "s87", "s88",
        "s89", "s90", "s91", "s92", "s93", "s94", "s95", "vcc", "scc", "memory");
}
}

extern "C" void kernel_launch(void* const* d_in, const int* in_sizes, int n_in, void* d_out, int out_size, void* d_ws, size_t ws_size,
                              hipStream_t stream) {
  constexpr unsigned WB = D * F * 2, XB = TOKENS * D * 2;
  mod_ffn_kernel<<<GRID, 256, 0, stream>>>(d_in[2], WB, SRD3, d_in[3], WB, SRD3, d_in[0], XB, SRD3, d_in[4], WB, SRD3, d_ws, WS_BYTES, SRD3,
                                           d_out, XB, SRD3, d_in[1], D * 2, SRD3);
}
```
